```python
import torch

def _softmax(x, axis=-1):
    return torch.softmax(x, dim=axis)

def _gelu(x):
    return torch.nn.functional.gelu(x, approximate='tanh')

def _silu(x):
    return torch.nn.functional.silu(x)

def _sigmoid(x):
    return torch.sigmoid(x)

def _layernorm(x, axis=-1, eps=1e-05):
    m = x.mean(dim=axis, keepdim=True)
    v = x.var(dim=axis, keepdim=True, unbiased=False)
    return (x - m) / torch.sqrt(v + eps)

def _scatter_add(tgt, idx, src, axis=0):
    return tgt.clone().index_add_(axis, idx, src)

def _segment_sum(src, seg, n):
    out = torch.zeros((n,) + src.shape[1:], dtype=src.dtype)
    return out.index_add_(0, seg, src)

def _pad(x, pw):
    flat = tuple((v for pair in reversed(pw) for v in pair))
    return torch.nn.functional.pad(x, flat)
kernel_name = 'gated_mix__softmax_full_mha__mixer_gmlp__prompt'

def setup_inputs():

    _w = lambda a: (lambda t: t.to(torch.bfloat16) if t.is_floating_point() else t)(torch.as_tensor(a))

    def _A_setup():
        B, S, D = (1, 2048, 640)
        return {'x': _w((lambda *s: torch.randn(*s, dtype=torch.bfloat16) * 0.05)(B, S, D)), 'wqkv': _w((lambda *s: torch.randn(*s, dtype=torch.bfloat16) * 0.8)(D, 3 * D)), 'wo': _w((lambda *s: torch.randn(*s, dtype=torch.bfloat16) * 0.1)(D, D))}
    _A_in = _A_setup()

    def _B_setup():
        B, S, D, F = (1, 2048, 640, 1728)
        Fh = F // 2
        return {'x': _w((lambda *s: torch.randn(*s, dtype=torch.bfloat16) * 0.05)(B, S, D)), 'w_in': _w((lambda *s: torch.randn(*s, dtype=torch.bfloat16) * 0.4)(D, 2 * Fh)), 'w_sp': _w(0.01 * (lambda *s: torch.randn(*s, dtype=torch.bfloat16) * 0.4)(S, S)), 'b_sp': _w(1.0 + 0.01 * (lambda *s: torch.randn(*s, dtype=torch.bfloat16) * 0.05)(S)), 'w_out': _w((lambda *s: torch.randn(*s, dtype=torch.bfloat16) * 0.05)(Fh, D))}
    _B_in = _B_setup()
    return {'A_x': _A_in['x'], 'A_wqkv': _A_in['wqkv'], 'A_wo': _A_in['wo'], 'B_x': _B_in['x'], 'B_w_in': _B_in['w_in'], 'B_w_sp': _B_in['w_sp'], 'B_b_sp': _B_in['b_sp'], 'B_w_out': _B_in['w_out'], 'gate_w': _w((lambda *s: torch.randn(*s, dtype=torch.bfloat16) * 0.05)(640) * 16.0)}

def reference(A_x, A_wqkv, A_wo, B_x, B_w_in, B_w_sp, B_b_sp, B_w_out, gate_w):

    def _A_ref(x, wqkv, wo):
        B, S, D = x.shape
        H = 32
        Dh = D // H
        qkv = (lambda x, *d: x.permute(*d))((x @ wqkv).reshape(B, S, 3, H, Dh), 2, 0, 3, 1, 4)
        q, k, v = (qkv[0], qkv[1], qkv[2])
        scale = Dh ** (-0.5)
        scores = torch.einsum('bhqd,bhkd->bhqk', q, k) * scale
        p = _softmax(scores, axis=-1)
        o = torch.einsum('bhqk,bhkd->bhqd', p, v)
        return (lambda x, *d: x.permute(*d))(o, 0, 2, 1, 3).reshape(B, S, D) @ wo

    def _B_ref(x, w_in, w_sp, b_sp, w_out):
        B, S, D = x.shape
        F = w_in.shape[-1]
        Fh = F // 2
        z = _gelu(torch.einsum('bsd,df->bsf', x, w_in))
        z1 = z[..., :Fh].clone()
        z2 = _layernorm(z[..., Fh:], axis=-1)
        g = torch.einsum('ts,bsf->btf', w_sp, z2) + b_sp[None, :, None]
        return torch.einsum('bsf,fd->bsd', z1 * g, w_out)
    x = A_x
    g = _sigmoid((x * gate_w).sum(-1, keepdims=True))
    a = _A_ref(x, A_wqkv, A_wo)
    b = _B_ref(x, B_w_in, B_w_sp, B_b_sp, B_w_out)
    return g * a + (1 - g) * b
```

```cpp
#include <hip/hip_runtime.h>
#include <hip/hip_bf16.h>
typedef __hip_bfloat16 bf16;
constexpr int S=2048,D=640,H=32,DH=20,F=1728,FH=864;
__device__ inline float b2f(bf16 x){return __bfloat162float(x);}
template<typename TA> __global__ void gemm(const TA* A,int lda,const bf16* B,int ldb,float* C,int ldc,int K){
  __shared__ float As[16][17],Bs[16][17];
  int tx=threadIdx.x&15,ty=threadIdx.x>>4; int r=blockIdx.y*16+ty,c=blockIdx.x*16+tx; float acc=0;
  for(int k=0;k<K;k+=16){ As[ty][tx]=(float)A[(size_t)r*lda+k+tx]; Bs[ty][tx]=b2f(B[(size_t)(k+ty)*ldb+c]); __syncthreads();
    for(int j=0;j<16;j++) acc+=As[ty][j]*Bs[j][tx]; __syncthreads(); }
  C[(size_t)r*ldc+c]=acc;
}
template<typename TA> __global__ void gemmf(const TA* A,int lda,const float* B,int ldb,float* C,int ldc,int K){
  __shared__ float As[16][17],Bs[16][17];
  int tx=threadIdx.x&15,ty=threadIdx.x>>4; int r=blockIdx.y*16+ty,c=blockIdx.x*16+tx; float acc=0;
  for(int k=0;k<K;k+=16){ As[ty][tx]=(float)A[(size_t)r*lda+k+tx]; Bs[ty][tx]=B[(size_t)(k+ty)*ldb+c]; __syncthreads();
    for(int j=0;j<16;j++) acc+=As[ty][j]*Bs[j][tx]; __syncthreads(); }
  C[(size_t)r*ldc+c]=acc;
}
__global__ void attn(const float* qkv,float* o){
  int h=blockIdx.y; int q=blockIdx.x*64+threadIdx.x; float qv[DH],acc[DH];
  for(int d=0;d<DH;d++){qv[d]=qkv[(size_t)q*1920+h*DH+d]*0.22360679775f;acc[d]=0;}
  float m=-1e30f,l=0;
  for(int k=0;k<S;k++){ float s=0; for(int d=0;d<DH;d++) s+=qv[d]*qkv[(size_t)k*1920+640+h*DH+d];
    float mn=fmaxf(m,s); float a=__expf(m-mn),p=__expf(s-mn); l=l*a+p; for(int d=0;d<DH;d++) acc[d]=acc[d]*a+p*qkv[(size_t)k*1920+1280+h*DH+d]; m=mn; }
  for(int d=0;d<DH;d++) o[(size_t)q*640+h*DH+d]=acc[d]/l;
}
__global__ void geluln(float* z){
  int r=blockIdx.x; float* zr=z+(size_t)r*F; __shared__ float red[256];
  float s=0; for(int i=threadIdx.x;i<F;i+=256){ float v=zr[i]; v=0.5f*v*(1.f+tanhf(0.7978845608f*(v+0.044715f*v*v*v))); zr[i]=v; if(i>=FH) s+=v; }
  red[threadIdx.x]=s; __syncthreads(); for(int o=128;o;o>>=1){ if(threadIdx.x<o) red[threadIdx.x]+=red[threadIdx.x+o]; __syncthreads(); }
  float mu=red[0]/FH; __syncthreads(); s=0; for(int i=threadIdx.x;i<F;i+=256) if(i>=FH){float d=zr[i]-mu; s+=d*d;}
  red[threadIdx.x]=s; __syncthreads(); for(int o=128;o;o>>=1){ if(threadIdx.x<o) red[threadIdx.x]+=red[threadIdx.x+o]; __syncthreads(); }
  float rs=rsqrtf(red[0]/FH+1e-5f); for(int i=threadIdx.x;i<F;i+=256) if(i>=FH) zr[i]=(zr[i]-mu)*rs;
}
__global__ void mulz(const float* z,float* g,const bf16* bsp){ int r=blockIdx.x; for(int i=threadIdx.x;i<FH;i+=256) g[(size_t)r*FH+i]=(g[(size_t)r*FH+i]+b2f(bsp[r]))*z[(size_t)r*F+i]; }
__global__ void fin(const bf16* x,const bf16* gw,const float* a,const float* b,bf16* out){ int r=blockIdx.x; __shared__ float red[256];
  float s=0; for(int i=threadIdx.x;i<D;i+=256) s+=b2f(x[(size_t)r*D+i])*b2f(gw[i]);
  red[threadIdx.x]=s; __syncthreads(); for(int o=128;o;o>>=1){ if(threadIdx.x<o) red[threadIdx.x]+=red[threadIdx.x+o]; __syncthreads(); }
  float g=1.f/(1.f+__expf(-red[0])); for(int i=threadIdx.x;i<D;i+=256) out[(size_t)r*D+i]=__float2bfloat16(g*a[(size_t)r*D+i]+(1.f-g)*b[(size_t)r*D+i]); }
extern "C" void kernel_launch(void* const* d_in, const int* in_sizes, int n_in, void* d_out, int out_size, void* d_ws, size_t ws_size, hipStream_t st) {
  const bf16* x=(const bf16*)d_in[0]; const bf16* wqkv=(const bf16*)d_in[1]; const bf16* wo=(const bf16*)d_in[2];
  const bf16* win=(const bf16*)d_in[4]; const bf16* wsp=(const bf16*)d_in[5]; const bf16* bsp=(const bf16*)d_in[6]; const bf16* wout=(const bf16*)d_in[7]; const bf16* gw=(const bf16*)d_in[8];
  float* ws=(float*)d_ws; float* qkv=ws; float* z=qkv+(size_t)S*1920; float* o=z+(size_t)S*F; float* g=o+(size_t)S*D; float* a=g+(size_t)S*FH; float* b=a+(size_t)S*D;
  gemm<bf16><<<dim3(1920/16,S/16),256,0,st>>>(x,D,wqkv,1920,qkv,1920,D);
  gemm<bf16><<<dim3(F/16,S/16),256,0,st>>>(x,D,win,F,z,F,D);
  attn<<<dim3(S/64,H),64,0,st>>>(qkv,o);
  geluln<<<S,256,0,st>>>(z);
  gemmf<bf16><<<dim3(FH/16,S/16),256,0,st>>>(wsp,S,z+FH,F,g,FH,S);
  mulz<<<S,256,0,st>>>(z,g,bsp);
  gemm<float><<<dim3(D/16,S/16),256,0,st>>>(o,D,wo,D,a,D,D);
  gemm<float><<<dim3(D/16,S/16),256,0,st>>>(g,FH,wout,D,b,D,FH);
  fin<<<S,256,0,st>>>(x,gw,a,b,(bf16*)d_out);
}
```

```cpp
#include <hip/hip_runtime.h>
#include <hip/hip_bf16.h>

typedef __attribute__((ext_vector_type(8))) __bf16 bf16x8;
typedef __attribute__((ext_vector_type(2))) __bf16 bf16x2;
typedef __attribute__((ext_vector_type(4))) short s16x4;
typedef __attribute__((ext_vector_type(8))) short s16x8;
typedef __attribute__((ext_vector_type(2))) float f32x2;
typedef __attribute__((ext_vector_type(4))) float f32x4;
typedef __attribute__((ext_vector_type(16))) float f32x16;
typedef __attribute__((ext_vector_type(2))) unsigned u32x2;
typedef __attribute__((ext_vector_type(4))) unsigned u32x4;
typedef __amdgpu_buffer_rsrc_t rsrc_t;
#define LAS __attribute__((address_space(3)))
#define GAS __attribute__((address_space(1)))
#define DI __device__ __forceinline__

constexpr int S = 2048, D = 640, FH = 864;
constexpr unsigned PLANE = S * 16;
constexpr unsigned QKV_BYTES = 240 * PLANE;
constexpr unsigned ZROW = FH * 16;
constexpr unsigned Z_BYTES = 256 * ZROW;
constexpr unsigned OU_BYTES = 188 * PLANE;
constexpr unsigned WT_TILE = 188 * 1280;
constexpr unsigned WT_BYTES = 8 * WT_TILE;
constexpr unsigned OFF_QKV = 0;
constexpr unsigned OFF_Z2R = OFF_QKV + QKV_BYTES;
constexpr unsigned OFF_Z2N = OFF_Z2R + Z_BYTES;
constexpr unsigned OFF_OU = OFF_Z2N + Z_BYTES;
constexpr unsigned OFF_WT = OFF_OU + OU_BYTES;
constexpr unsigned OFF_GATE = OFF_WT + WT_BYTES;
constexpr unsigned OFF_FLAG = OFF_GATE + S * 4;

DI int pi32(int r) { return (r & 0x13) | ((r & 4) << 1) | ((r & 8) >> 1); }
DI rsrc_t mk_rsrc(const void* p, unsigned bytes) { return __builtin_amdgcn_make_buffer_rsrc((void*)p, (short)0, (int)bytes, 0x00020000); }
DI unsigned rfl(unsigned v) { return __builtin_amdgcn_readfirstlane(v); }
DI void dma16(unsigned m0v, unsigned voff, rsrc_t srd, unsigned soff) {
  asm volatile("s_mov_b32 m0, %0\n\ts_nop 0\n\tbuffer_load_dwordx4 %1, %2, %3 offen lds" ::"s"(m0v), "v"(voff), "s"(srd), "s"(soff) : "memory");
}
DI void dma16_sc1(unsigned m0v, unsigned voff, rsrc_t srd, unsigned soff) {
  asm volatile("s_mov_b32 m0, %0\n\ts_nop 0\n\tbuffer_load_dwordx4 %1, %2, %3 offen sc1 lds" ::"s"(m0v), "v"(voff), "s"(srd), "s"(soff) : "memory");
}
template <int N> DI void wait_vm() { asm volatile("s_waitcnt vmcnt(%0)" ::"i"(N) : "memory"); }
DI void bar() { asm volatile("s_barrier" ::: "memory"); }
DI void bar_lgkm() { asm volatile("s_waitcnt lgkmcnt(0)\n\ts_barrier" ::: "memory"); }
#define SB() __builtin_amdgcn_sched_barrier(0)
typedef const LAS char* lptr;
DI bf16x8 ld128(lptr p) { return *(const LAS bf16x8*)p; }
DI s16x4 ld64(lptr p) { return *(const LAS s16x4*)p; }
DI s16x4 ldtr(lptr p) { return __builtin_amdgcn_ds_read_tr16_b64_v4i16((LAS s16x4*)p); }
DI bf16x8 cat8(s16x4 a, s16x4 b) { return __builtin_bit_cast(bf16x8, (s16x8)__builtin_shufflevector(a, b, 0, 1, 2, 3, 4, 5, 6, 7)); }
DI bf16x8 ldtr8(lptr p, int off2) { return cat8(ldtr(p), ldtr(p + off2)); }
DI f32x16 mfma16(bf16x8 a, bf16x8 b, f32x16 c) { return __builtin_amdgcn_mfma_f32_32x32x16_bf16(a, b, c, 0, 0, 0); }
DI f32x16 mfma8(s16x4 a, s16x4 b, f32x16 c) { return __builtin_amdgcn_mfma_f32_32x32x8bf16_1k(a, b, c, 0, 0, 0); }
DI unsigned pk2(float a, float b) { f32x2 v = {a, b}; return __builtin_bit_cast(unsigned, __builtin_convertvector(v, bf16x2)); }
DI u32x4 pk8(const f32x16& a, int o) { u32x4 r = {pk2(a[o], a[o + 1]), pk2(a[o + 2], a[o + 3]), pk2(a[o + 4], a[o + 5]), pk2(a[o + 6], a[o + 7])}; return r; }
DI float gelu(float v) {
  const float k1 = -2.0f * 0.7978845608028654f * 1.4426950408889634f, k2 = k1 * 0.044715f;
  const float e = __builtin_amdgcn_exp2f(v * __builtin_fmaf(v * v, k2, k1));
  return v * __builtin_amdgcn_rcpf(1.0f + e);
}
DI void st128(rsrc_t r, unsigned voff, u32x4 v) { __builtin_amdgcn_raw_buffer_store_b128(v, r, voff, 0, 0); }

#define PROF_DECL
#define STAMP(j)
#define CSTAMP(j)
#define PROF_OUT(k, lo, hi)
constexpr int P_STAGE = 40960, P_WQ = 16384, P_WZ = 32768;

template <bool GATE>
DI void proj_body(char* lds, const void* x, const void* wqkv, const void* win, const void* gw, char* ws, int panel, int ct) {
  const int tid = threadIdx.x, l = tid & 63, w = rfl(tid >> 6), wm = w & 3, wn = w >> 2;
  const int r = l & 31, h = l >> 5;
  PROF_DECL; STAMP(0);
  const rsrc_t rx = mk_rsrc(x, S * D * 2), rq = mk_rsrc(wqkv, D * 1920 * 2);
  const rsrc_t rz = GATE ? mk_rsrc(gw, D * 2) : mk_rsrc(win, D * 1728 * 2);
  const unsigned lds0 = (unsigned)(size_t)lds;
  unsigned vx[2];
#pragma unroll
  for (int i = 0; i < 2; ++i) { const int row = 16 * w + 8 * i + (l >> 3); vx[i] = (panel * 128 + row) * 1280 + (((l & 7) ^ ((row >> 1) & 7)) << 4); }
  const unsigned vq = (8 * w + (l >> 4)) * 3840 + ct * 256 + (((l & 15) ^ (((l >> 4) & 3) << 2)) << 4);
  const unsigned vz = GATE ? (l & 7) << 4 : (8 * w + (l >> 3)) * 3456 + (832 + 64 * ct) * 2 + (((l & 7) ^ (((l >> 4) & 1) << 2)) << 4);
  auto issue = [&](int st) {
    const unsigned b = lds0 + (st & 3) * P_STAGE;
    dma16(b + w * 2048, vx[0], rx, st * 128);
    dma16(b + w * 2048 + 1024, vx[1], rx, st * 128);
    dma16(b + P_WQ + w * 2048, vq, rq, st * 64 * 3840);
    dma16(b + P_WQ + w * 2048 + 1024, vq, rq, st * 64 * 3840 + 4 * 3840);
    dma16(b + P_WZ + w * 1024, vz, rz, GATE ? st * 128 : st * 64 * 3456);
  };
  issue(0); issue(1); issue(2);
  const int xrow = 32 * wm + pi32(r);
  unsigned ax[4];
#pragma unroll
  for (int ks = 0; ks < 4; ++ks) ax[ks] = xrow * 128 + (((2 * ks + h) ^ ((xrow >> 1) & 7)) << 4);
  const int q = (l >> 2) & 3, cq = pi32((l & 16) + 4 * (l & 3));
  unsigned aq[2];
#pragma unroll
  for (int t = 0; t < 2; ++t) { const int col = 64 * wn + 32 * t + cq; aq[t] = P_WQ + (8 * h + q) * 256 + (((col >> 3) ^ (q << 2)) << 4) + (col & 4) * 2; }
  const int zc = 32 * wn + cq;
  const unsigned az = GATE ? P_WZ + h * 16 : P_WZ + (8 * h + q) * 128 + (((zc >> 3) ^ ((q >> 1) << 2)) << 4) + (zc & 4) * 2;
  f32x16 c0 = {}, c1 = {}, cz = {};
#pragma unroll
  for (int st = 0; st < 10; ++st) {
    if (st < 8) wait_vm<10>(); else if (st == 8) wait_vm<5>(); else wait_vm<0>();
    bar_lgkm();
    if (st == 0) STAMP(1);
    if (st == 3) STAMP(2);
    if (st + 3 < 10) issue(st + 3);
    const lptr b = (lptr)lds + (st & 3) * P_STAGE;
#pragma unroll
    for (int ks = 0; ks < 4; ++ks) {
      const bf16x8 xf = ld128(b + ax[ks]);
      const bf16x8 w0 = ldtr8(b + aq[0] + ks * 4096, 1024), w1 = ldtr8(b + aq[1] + ks * 4096, 1024);
      const bf16x8 wz = GATE ? ld128(b + az + ks * 32) : ldtr8(b + az + ks * 2048, 512);
      c0 = mfma16(w0, xf, c0);
      c1 = mfma16(w1, xf, c1);
      cz = mfma16(xf, wz, cz);
    }
  }
  STAMP(3);
  const rsrc_t ro = mk_rsrc(ws + OFF_QKV, QKV_BYTES);
  if (ct < 5) { const float sc = 0.22360679774997896f * 1.4426950408889634f; c0 *= sc; c1 *= sc; }
  const unsigned po = (ct * 16 + wn * 8 + h) * PLANE + (panel * 128 + wm * 32 + r) * 16;
  st128(ro, po, pk8(c0, 0)); st128(ro, po + 2 * PLANE, pk8(c0, 8));
  st128(ro, po + 4 * PLANE, pk8(c1, 0)); st128(ro, po + 6 * PLANE, pk8(c1, 8));
  if (GATE) {
    if (wn == 0 && r == 0) {
      float* g = (float*)(ws + OFF_GATE) + panel * 128 + wm * 32 + 8 * h;
#pragma unroll
      for (int i = 0; i < 16; ++i) g[(i & 7) + 2 * (i & 8)] = __builtin_amdgcn_rcpf(1.0f + __builtin_amdgcn_exp2f(-1.4426950408889634f * cz[i]));
    }
  } else {
    const int ft = 2 * ct + wn - 1;
    if (ft >= 0) {
#pragma unroll
      for (int i = 0; i < 16; ++i) cz[i] = gelu(cz[i]);
      const rsrc_t rzo = mk_rsrc(ws + OFF_Z2R, Z_BYTES);
      const unsigned zo = (panel * 16 + wm * 4 + h) * ZROW + (ft * 32 + r) * 16;
      st128(rzo, zo, pk8(cz, 0)); st128(rzo, zo + 2 * ZROW, pk8(cz, 8));
    }
  }
}

DI void pack_weights(const void* wo, const void* wout, char* ws, int pb) {
  const int tid = threadIdx.x;
  PROF_DECL; STAMP(0);
  if (pb == 0 && tid < 16) *(unsigned*)(ws + OFF_FLAG + tid * 128) = 0u;
  const rsrc_t r0 = mk_rsrc(wo, D * D * 2), r1 = mk_rsrc(wout, FH * D * 2), rw = mk_rsrc(ws + OFF_WT, WT_BYTES);
  u32x2 v[4][8];
#pragma unroll
  for (int it = 0; it < 4; ++it) {
    const int item = (it * 16 + pb) * 512 + tid, P = item / 160, nq = item % 160;
#pragma unroll
    for (int k = 0; k < 8; ++k) {
      const unsigned o0 = P < 80 ? (P * 8 + k) * 1280 + nq * 8 : 0xF0000000u, o1 = P >= 80 ? ((P - 80) * 8 + k) * 1280 + nq * 8 : 0xF0000000u;
      v[it][k] = __builtin_amdgcn_raw_buffer_load_b64(r0, o0, 0, 0) | __builtin_amdgcn_raw_buffer_load_b64(r1, o1, 0, 0);
    }
  }
#pragma unroll
  for (int it = 0; it < 4; ++it) {
    const int item = (it * 16 + pb) * 512 + tid, P = item / 160, nq = item % 160;
    const int nt = nq / 20, ql = nq % 20, g = ql / 5, j = ql % 5;
    const unsigned o = P < 188 ? nt * WT_TILE + P * 1280 + (16 * j + 4 * g) * 16 : 0xF0000000u;
#pragma unroll
    for (int e = 0; e < 4; ++e) {
      const unsigned sel = (e & 1) ? 0x07060302u : 0x05040100u;
      u32x4 t;
#pragma unroll
      for (int k = 0; k < 4; ++k) t[k] = __builtin_amdgcn_perm(v[it][2 * k + 1][e >> 1], v[it][2 * k][e >> 1], sel);
      st128(rw, o + e * 16, t);
    }
  }
}

__global__ __launch_bounds__(512) void k_proj(const void* x, const void* wqkv, const void* win, const void* gw, const void* wo, const void* wout, char* ws) {
  __shared__ __attribute__((aligned(16))) char lds[4 * P_STAGE];
  const int xcd = blockIdx.x & 7, idx = blockIdx.x >> 3, i = xcd >> 2, j = xcd & 3;
  const int panel = 8 * i + (idx & 7), ct = 4 * j + (idx >> 3);
  if (ct == 15) pack_weights(wo, wout, ws, panel);
  else if (ct == 14) proj_body<true>(lds, x, wqkv, win, gw, ws, panel, ct);
  else proj_body<false>(lds, x, wqkv, win, gw, ws, panel, ct);
}

constexpr int M_TR = 0, M_FR = 65536, M_KV = 98304, KV_SLOT = 3776, KV_ONES = 3264, M_LN = M_KV + 6 * KV_SLOT, M_BYTES = M_LN + 256;

struct MixState {
  f32x16 sa, sb, o, g;
  bf16x8 qa, ka, v0, v1, p0, p1, fa, ta, fb, tb;
  s16x4 qc, kc;
  lptr pk, pkc, pv, pf, pw;
  lptr pt[4];
};
#define PIN(x) asm volatile("" : "+v"(x))
DI void exp2x2(f32x16& s, int i) { s[i] = __builtin_amdgcn_exp2f(s[i]); s[i + 1] = __builtin_amdgcn_exp2f(s[i + 1]); }
DI void setw(bf16x8& p, int k, unsigned v) { u32x4 t = __builtin_bit_cast(u32x4, p); t[k] = v; p = __builtin_bit_cast(bf16x8, t); }

template <int PH, int GS, int KS> DI void g_read(MixState& m, bf16x8& f, bf16x8& t) {
  t = ld128(m.pt[KS] + GS * 16384);
  if (PH == 1) f = ldtr8(m.pw + GS * 8192 + KS * 2048, 512); else f = ld128(m.pf + GS * 8192 + KS * 2048);
}
template <int PH, int PHN, int MODE, int E, int GS, bool ENDBAR = true> DI void att_iter(MixState& m, f32x16& cur, f32x16& nxt, bool first) {
  constexpr int EO = E * KV_SLOT, EN = ((E + 1) % 6) * KV_SLOT, GN = (GS + 1) & 3;
  const f32x16 z = {};
  SB();
  m.v0 = ldtr8(m.pv + EO, 128);
  if (MODE == 1) g_read<PH, GS, 0>(m, m.fa, m.ta);
  if (MODE == 3) g_read<PH, GS, 2>(m, m.fa, m.ta);
  SB();
  nxt = mfma16(m.ka, m.qa, z); exp2x2(cur, 0); PIN(cur); SB();
  if (MODE == 1) g_read<PH, GS, 1>(m, m.fb, m.tb);
  if (MODE == 2) m.g = mfma16(m.fb, m.tb, m.g);
  if (MODE == 3) m.g = mfma16(m.fb, m.tb, m.g);
  setw(m.p0, 0, pk2(cur[0], cur[1])); exp2x2(cur, 2); PIN(cur); PIN(m.p0); SB();
  if (!first) m.o = mfma16(m.v1, m.p1, m.o);
  if (MODE == 3) g_read<PH, GS, 3>(m, m.fb, m.tb);
  setw(m.p0, 1, pk2(cur[2], cur[3])); exp2x2(cur, 4); PIN(cur); PIN(m.p0); SB();
  nxt = mfma8(m.kc, m.qc, nxt);
  setw(m.p0, 2, pk2(cur[4], cur[5])); exp2x2(cur, 6); PIN(cur); PIN(m.p0); SB();
  if (MODE == 2) { bar_lgkm(); SB(); g_read<PHN, GN, 0>(m, m.fa, m.ta); }
  m.v1 = ldtr8(m.pv + EO + 256, 128);
  m.ka = ld128(m.pk + EN); m.kc = ld64(m.pkc + EN);
  SB();
  if (MODE == 1) m.g = mfma16(m.fa, m.ta, m.g);
  if (MODE == 3) m.g = mfma16(m.fa, m.ta, m.g);
  setw(m.p0, 3, pk2(cur[6], cur[7])); exp2x2(cur, 8); PIN(cur); PIN(m.p0); SB();
  if (MODE == 1) g_read<PH, GS, 2>(m, m.fa, m.ta);
  if (MODE == 2) g_read<PHN, GN, 1>(m, m.fb, m.tb);
  m.o = mfma16(m.v0, m.p0, m.o);
  setw(m.p1, 0, pk2(cur[8], cur[9])); exp2x2(cur, 10); PIN(cur); PIN(m.p1); SB();
  if (MODE == 1) m.g = mfma16(m.fb, m.tb, m.g);
  if (MODE == 3) m.g = mfma16(m.fb, m.tb, m.g);
  setw(m.p1, 1, pk2(cur[10], cur[11])); exp2x2(cur, 12); PIN(cur); PIN(m.p1); SB();
  if (MODE == 1) { g_read<PH, GS, 3>(m, m.fb, m.tb); m.g = mfma16(m.fa, m.ta, m.g); }
  if (MODE == 2) m.g = mfma16(m.fa, m.ta, m.g);
  exp2x2(cur, 14); setw(m.p1, 2, pk2(cur[12], cur[13])); setw(m.p1, 3, pk2(cur[14], cur[15])); PIN(m.p1); SB();
  if (ENDBAR && (MODE == 3 || MODE == 0)) bar_lgkm();
}
template <int PH, int E, int GS> DI void att_triple(MixState& m, f32x16& a, f32x16& b) {
  att_iter<PH, PH, 1, E, GS>(m, a, b, false);
  att_iter<PH, PH, 2, (E + 1) % 6, GS>(m, b, a, false);
  att_iter<PH, PH, 3, (E + 2) % 6, (GS + 1) & 3>(m, a, b, false);
}

DI f32x16 att_fallback(const MixState& m, char* wl, const char* qkv, int oa, int ob, int oc, int hoff, int l) {
  const int r = l & 31, h = l >> 5;
  for (int i = l; i < 128; i += 64) *(LAS unsigned*)((LAS char*)wl + KV_ONES + i * 4) = 0x3F803F80u;
  const int q = (l >> 2) & 3, p = l & 3;
  const lptr pv = (lptr)wl + ((l & 16) ? (p == 0 ? 2 * 1088 + 512 + hoff : KV_ONES) : (p & 1) * 1088 + 512 + (p >> 1) * 8) + h * 64 + q * 16;
  f32x16 o = {};
  float mx = -3.0e38f;
  for (int t = 0; t < 64; ++t) {
    const unsigned so = (t * 32 + r) * 16;
    const bf16x8 ka = *(const bf16x8*)(qkv + (size_t)(80 + (h ? ob : oa)) * PLANE + so);
    const s16x4 kc = *(const s16x4*)(qkv + (size_t)(80 + oc) * PLANE + so + hoff);
    const u32x4 va = *(const u32x4*)(qkv + (size_t)(160 + (h ? ob : oa)) * PLANE + so);
    const u32x4 vc = *(const u32x4*)(qkv + (size_t)(160 + oc) * PLANE + so);
    *(LAS u32x4*)((LAS char*)wl + h * 1088 + 512 + r * 16) = va;
    *(LAS u32x4*)((LAS char*)wl + 2 * 1088 + 512 + r * 16) = vc;
    asm volatile("s_waitcnt lgkmcnt(0)" ::: "memory");
    const f32x16 z = {};
    f32x16 s = mfma8(kc, m.qc, mfma16(ka, m.qa, z));
    float tm = s[0];
#pragma unroll
    for (int i = 1; i < 16; ++i) tm = __builtin_fmaxf(tm, s[i]);
    tm = __builtin_fmaxf(tm, __shfl_xor(tm, 32));
    const float mn = __builtin_fmaxf(mx, tm), al = __builtin_amdgcn_exp2f(mx - mn);
    mx = mn;
#pragma unroll
    for (int i = 0; i < 16; ++i) { s[i] = __builtin_amdgcn_exp2f(s[i] - mn); o[i] *= al; }
    const bf16x8 v0 = ldtr8(pv, 128), v1 = ldtr8(pv + 256, 128);
    o = mfma16(v0, __builtin_bit_cast(bf16x8, pk8(s, 0)), o);
    o = mfma16(v1, __builtin_bit_cast(bf16x8, pk8(s, 8)), o);
    asm volatile("s_waitcnt lgkmcnt(0)" ::: "memory");
  }
  return o;
}

DI void mix_compute(char* lds, const void* bsp, char* ws, int w, int head, int qb, int tp, int FT, bool hasg) {
  const int l = threadIdx.x & 63, r = l & 31, h = l >> 5;
  const int odd = head & 1, o0 = 5 * (head >> 1), oa = o0 + (odd ? 3 : 0), ob = oa + 1, oc = o0 + 2, hoff = odd * 8;
  const char* qkv = ws + OFF_QKV;
  PROF_DECL; STAMP(0);
  MixState m;
  const unsigned qs = (qb * 256 + w * 32 + r) * 16;
  m.qa = *(const bf16x8*)(qkv + (size_t)(h ? ob : oa) * PLANE + qs);
  m.qc = *(const s16x4*)(qkv + (size_t)oc * PLANE + qs + hoff);
  if (h) m.qc = s16x4{0, 0, 0, 0};
  const int ft = w & 1, tt = w >> 1;
  const int ttok = tp * 128 + tt * 32 + pi32(r), qtok = qb * 256 + w * 32 + pi32(r);
  const float gq = ((const float*)(ws + OFF_GATE))[qtok], gt = ((const float*)(ws + OFF_GATE))[ttok];
  const float bs = __builtin_bit_cast(float, (unsigned)((const unsigned short*)bsp)[ttok] << 16);
  for (int i = threadIdx.x; i < 6 * 128; i += 512) *(LAS unsigned*)((LAS char*)lds + M_KV + (i >> 7) * KV_SLOT + KV_ONES + (i & 127) * 4) = 0x3F803F80u;
  const lptr lb = (lptr)lds;
  const int q = (l >> 2) & 3, p = l & 3;
  m.pk = lb + M_KV + h * 1088 + r * 16;
  m.pkc = lb + M_KV + 2 * 1088 + r * 16 + hoff;
  m.pv = lb + M_KV + ((l & 16) ? (p == 0 ? 2 * 1088 + 512 + hoff : KV_ONES) : (p & 1) * 1088 + 512 + (p >> 1) * 8) + h * 64 + q * 16;
  const int trow = 32 * tt + pi32(r);
#pragma unroll
  for (int ks = 0; ks < 4; ++ks) m.pt[ks] = lb + M_TR + trow * 128 + (((2 * ks + h) ^ ((trow >> 1) & 7)) << 4);
  m.pf = lb + M_FR + h * 1024 + (32 * ft + r) * 16;
  const int zc = 32 * ft + pi32((l & 16) + 4 * p);
  m.pw = lb + M_FR + (8 * h + q) * 128 + (((zc >> 3) ^ ((q >> 1) << 2)) << 4) + (zc & 4) * 2;
  m.o = f32x16{}; m.g = f32x16{};
  m.p1 = bf16x8{}; m.v1 = bf16x8{}; m.p0 = bf16x8{};
  bar_lgkm();
  STAMP(1);
  {
    const f32x16 z = {};
    m.ka = ld128(m.pk); m.kc = ld64(m.pkc);
    m.sa = mfma8(m.kc, m.qc, mfma16(m.ka, m.qa, z));
    m.ka = ld128(m.pk + KV_SLOT); m.kc = ld64(m.pkc + KV_SLOT);
  }
  att_iter<1, 1, 0, 1, 0>(m, m.sa, m.sb, true);
  STAMP(2);
  att_triple<1, 2, 0>(m, m.sb, m.sa);
  att_triple<1, 5, 2>(m, m.sa, m.sb);
  STAMP(3);
  att_triple<1, 2, 0>(m, m.sb, m.sa);
  att_triple<1, 5, 2>(m, m.sa, m.sb);
  att_iter<1, 1, 1, 2, 0>(m, m.sb, m.sa, false);
  att_iter<1, 1, 2, 3, 0>(m, m.sa, m.sb, false);
  att_iter<1, 1, 3, 4, 1, false>(m, m.sb, m.sa, false);
  f32x16 z1;
#pragma unroll
  for (int i = 0; i < 16; ++i) { z1[i] = gelu(m.g[i]) * (1.0f - gt); m.g[i] = bs; }
  PIN(z1); PIN(m.g);
  STAMP(4);
  bar_lgkm();
  STAMP(5); CSTAMP(14);
#pragma unroll 1
  for (int it = 0; it < 8; ++it) {
    att_triple<2, 5, 2>(m, m.sa, m.sb);
    att_triple<2, 2, 0>(m, m.sb, m.sa);
  }
  STAMP(6); CSTAMP(15);
  m.o = mfma16(m.v1, m.p1, m.o);
  const rsrc_t rou = mk_rsrc(ws + OFF_OU, OU_BYTES);
  if (hasg && 2 * FT + ft < 27) {
    const f32x16 u = z1 * m.g;
    const unsigned uo = (80 + 8 * FT + 4 * ft + h) * PLANE + (tp * 128 + tt * 32 + r) * 16;
    st128(rou, uo, pk8(u, 0)); st128(rou, uo + 2 * PLANE, pk8(u, 8));
  }
  float big = 0.f;
#pragma unroll
  for (int i = 0; i < 12; ++i) big = __builtin_fmaxf(big, __builtin_fabsf(m.o[i]));
  const bool ok = m.o[12] > 9.5367431640625e-07f && m.o[12] < 1.152921504606847e18f && big < 1.0e37f;
  if (__builtin_expect(!__all(ok), 0)) m.o = att_fallback(m, lds + w * KV_SLOT, qkv, oa, ob, oc, hoff, l);
  const float rl = gq * __builtin_amdgcn_rcpf(m.o[12]);
  const f32x16 o = m.o * rl;
  st128(rou, (h ? ob : oa) * PLANE + qs, pk8(o, 0));
  if (h == 0) { const u32x2 t = {pk2(o[8], o[9]), pk2(o[10], o[11])}; __builtin_amdgcn_raw_buffer_store_b64(t, rou, oc * PLANE + qs + hoff, 0, 0); }
}

DI float lo16(unsigned v) { return __builtin_bit_cast(float, v << 16); }
DI float hi16(unsigned v) { return __builtin_bit_cast(float, v & 0xFFFF0000u); }

template <int ROLE>
DI void mix_loader(char* lds, const void* x, const void* win, const void* wsp, char* ws, int head, int tp, int FT) {
  const int l = threadIdx.x & 63, lt = ROLE * 64 + l, blk = blockIdx.x;
  const unsigned lds0 = (unsigned)(size_t)lds;
  __builtin_amdgcn_s_setprio(3);
  PROF_DECL;
  const rsrc_t rzr = mk_rsrc(ws + OFF_Z2R + (size_t)blk * ZROW, ZROW);
  u32x4 zr[4], piv;
  asm volatile("s_nop 4\n\tbuffer_load_dwordx4 %0, %5, %7, 0 offen\n\tbuffer_load_dwordx4 %1, %5, %7, %8 offen\n\tbuffer_load_dwordx4 %2, %5, %7, %9 offen\n\t"
               "buffer_load_dwordx4 %3, %6, %7, 0 offen\n\tbuffer_load_dwordx4 %4, off, %7, 0"
               : "=&v"(zr[0]), "=&v"(zr[1]), "=&v"(zr[2]), "=&v"(zr[3]), "=&v"(piv)
               : "v"(lt * 16), "v"(lt < FH - 768 ? lt * 16 + 12288 : 0), "s"(rzr), "s"(4096), "s"(8192) : "memory");
  const rsrc_t r1 = ROLE == 0 ? mk_rsrc(ws + OFF_QKV, QKV_BYTES) : ROLE == 3 ? mk_rsrc(win, D * 1728 * 2) : mk_rsrc(x, S * D * 2);
  const rsrc_t r2 = ROLE == 3 ? mk_rsrc(ws + OFF_Z2N, Z_BYTES) : mk_rsrc(wsp, S * S * 2);
  unsigned va[3], vb[2];
  if (ROLE == 0) {
    const int odd = head & 1, o0 = 5 * (head >> 1);
#pragma unroll
    for (int i = 0; i < 3; ++i) {
      const int oct = i == 2 ? o0 + 2 : o0 + (odd ? 3 : 0) + i;
      va[i] = l < 32 ? (80 + oct) * PLANE + l * 16 : (160 + oct) * PLANE + (l - 32) * 16 - 512;
    }
  } else if (ROLE == 3) {
    va[0] = (l >> 3) * 3456 + FT * 128 + (((l & 7) ^ (((l >> 4) & 1) << 2)) << 4);
    vb[0] = (64 * FT + l < FH ? 64 * FT + l : 64 * FT + l - 32) * 16;
  } else {
#pragma unroll
    for (int i = 0; i < 2; ++i) {
      const int row = 64 * (ROLE - 1) + 8 * i + (l >> 3); const unsigned c = ((l & 7) ^ ((row >> 1) & 7)) << 4;
      va[i] = (tp * 128 + row) * 1280 + c; vb[i] = (tp * 128 + row) * 4096 + c;
    }
  }
  int e = 0, es = 0;
  auto kv = [&]() {
#pragma unroll
    for (int i = 0; i < 3; ++i) dma16(lds0 + M_KV + es * KV_SLOT + i * 1088, va[i], r1, e * 512);
    ++e; es = es == 5 ? 0 : es + 1;
  };
  auto stage1 = [&](int st) {
#pragma unroll
    for (int i = 0; i < 8; ++i) {
      if (ROLE == 3) dma16(lds0 + M_FR + (st & 3) * 8192 + i * 1024, va[0], r1, (st * 64 + i * 8) * 3456);
      else dma16(lds0 + M_TR + (st & 3) * 16384 + (ROLE - 1) * 8192 + i * 1024, va[i & 1], r1, st * 128 + (i >> 1) * 16 * 1280);
    }
  };
  auto stage2 = [&](int st) {
    const int k = st - 10;
#pragma unroll
    for (int i = 0; i < 8; ++i) {
      if (ROLE == 3) dma16_sc1(lds0 + M_FR + (st & 3) * 8192 + i * 1024, vb[0], r2, (k * 8 + i) * ZROW);
      else dma16(lds0 + M_TR + (st & 3) * 16384 + (ROLE - 1) * 8192 + i * 1024, vb[i & 1], r2, k * 128 + (i >> 1) * 16 * 4096);
    }
  };
  if (ROLE == 0) { kv(); kv(); kv(); kv(); kv(); kv(); wait_vm<9>(); }
  else { stage1(0); stage1(1); stage1(2); }
  bar();
  asm volatile("s_waitcnt vmcnt(%5)" : "+v"(zr[0]), "+v"(zr[1]), "+v"(zr[2]), "+v"(zr[3]), "+v"(piv) : "i"(ROLE == 0 ? 18 : 24) : "memory");
  float d[4][8], s[16];
#pragma unroll
  for (int t = 0; t < 8; ++t) { s[t] = 0.f; s[8 + t] = 0.f; }
#pragma unroll
  for (int i = 0; i < 4; ++i) {
#pragma unroll
    for (int t = 0; t < 8; ++t) {
      const float pv = (t & 1) ? hi16(piv[t >> 1]) : lo16(piv[t >> 1]), zv = (t & 1) ? hi16(zr[i][t >> 1]) : lo16(zr[i][t >> 1]);
      d[i][t] = zv - pv;
      s[t] += d[i][t]; s[8 + t] = __builtin_fmaf(d[i][t], d[i][t], s[8 + t]);
    }
  }
#pragma unroll
  for (int t = 0; t < 16; ++t) {
#pragma unroll
    for (int o = 1; o < 64; o <<= 1) s[t] += __shfl_xor(s[t], o);
  }
  if (l == 0) {
#pragma unroll
    for (int t = 0; t < 16; ++t) *(LAS float*)((LAS char*)lds + M_LN + ROLE * 64 + t * 4) = s[t];
  }
  wait_vm<ROLE == 0 ? 6 : 16>();
  bar_lgkm();
  if (ROLE == 0) { kv(); kv(); } else stage1(3);
  {
    const rsrc_t rzo = mk_rsrc(ws + OFF_Z2N + (size_t)blk * ZROW, ZROW);
    float mu[8], rs[8];
#pragma unroll
    for (int t = 0; t < 8; ++t) {
      float a = 0.f, b = 0.f;
#pragma unroll
      for (int k = 0; k < 4; ++k) { a += *(const LAS float*)((lptr)lds + M_LN + k * 64 + t * 4); b += *(const LAS float*)((lptr)lds + M_LN + k * 64 + 32 + t * 4); }
      mu[t] = a * (1.0f / FH);
      rs[t] = __builtin_amdgcn_rsqf(__builtin_fmaxf(b * (1.0f / FH) - mu[t] * mu[t], 0.f) + 1e-5f);
    }
#pragma unroll
    for (int i = 0; i < 4; ++i) {
      u32x4 o;
#pragma unroll
      for (int t = 0; t < 4; ++t) o[t] = pk2((d[i][2 * t] - mu[2 * t]) * rs[2 * t], (d[i][2 * t + 1] - mu[2 * t + 1]) * rs[2 * t + 1]);
      __builtin_amdgcn_raw_buffer_store_b128(o, rzo, lt * 16 + i * 4096, 0, 16);
    }
  }
  GAS unsigned* flag = (GAS unsigned*)(ws + OFF_FLAG);
  if (ROLE == 0) {
    wait_vm<10>(); bar(); kv();
    wait_vm<10>(); bar(); kv(); kv();
    wait_vm<6>();
    if (l == 0) __hip_atomic_fetch_add(flag + (blk >> 4) * 32, 1u, __ATOMIC_RELAXED, __HIP_MEMORY_SCOPE_AGENT);
    bar(); kv();
    wait_vm<4>(); bar(); kv(); kv();
#pragma unroll 1
    for (int s2 = 5; s2 < 39; s2 += 2) {
      wait_vm<6>(); bar(); kv();
      wait_vm<3>(); bar(); kv(); kv();
    }
    wait_vm<6>(); bar();
    wait_vm<3>(); bar();
    wait_vm<0>(); bar();
  } else {
    wait_vm<20>(); bar(); stage1(4);
    wait_vm<20>(); bar(); stage1(5);
    STAMP(9); wait_vm<16>(); STAMP(10);
    if (l == 0) __hip_atomic_fetch_add(flag + (blk >> 4) * 32, 1u, __ATOMIC_RELAXED, __HIP_MEMORY_SCOPE_AGENT);
    bar(); stage1(6);
    wait_vm<17>(); bar(); stage1(7);
    wait_vm<17>(); bar(); stage1(8);
    wait_vm<16>(); bar(); stage1(9);
    wait_vm<16>(); bar();
    if (ROLE == 3) {
      STAMP(11);
      for (unsigned spin = 0;; ++spin) {
        const unsigned c = __hip_atomic_load(flag + (l & 15) * 32, __ATOMIC_RELAXED, __HIP_MEMORY_SCOPE_AGENT);
        if (__all(c >= 64u) || spin > (1u << 20)) break;
        __builtin_amdgcn_s_sleep(1);
      }
      asm volatile("" ::: "memory");
      STAMP(12);
    }
    stage2(10);
#pragma unroll 1
    for (int s2 = 8; s2 < 39; ++s2) { wait_vm<16>(); bar(); stage2(s2 + 3); }
    wait_vm<16>(); bar();
    wait_vm<8>(); bar();
    wait_vm<0>(); bar();
  }
  bar();
  if (ROLE == 3) PROF_OUT(1, 9, 13);
}

__global__ __launch_bounds__(768) void k_mix(const void* x, const void* win, const void* wsp, const void* bsp, char* ws) {
  __shared__ __attribute__((aligned(16))) char lds[M_BYTES];
  const int xcd = blockIdx.x & 7, idx = blockIdx.x >> 3, w = rfl(threadIdx.x >> 6);
  const int head = 4 * xcd + (idx >> 3), qb = idx & 7;
  const bool hasg = idx < 28;
  const int gi = hasg ? idx : idx - 28, tp = 4 * (xcd >> 1) + (gi & 3), FT = 7 * (xcd & 1) + (gi >> 2);
  if (w < 8) mix_compute(lds, bsp, ws, w, head, qb, tp, FT, hasg);
  else if (w == 8) mix_loader<0>(lds, x, win, wsp, ws, head, tp, FT);
  else if (w == 9) mix_loader<1>(lds, x, win, wsp, ws, head, tp, FT);
  else if (w == 10) mix_loader<2>(lds, x, win, wsp, ws, head, tp, FT);
  else mix_loader<3>(lds, x, win, wsp, ws, head, tp, FT);
}

constexpr int O_STAGE = 36864, O_W = 16384;
__global__ __launch_bounds__(256) void k_out(char* ws, void* out) {
  PROF_DECL; STAMP(0);
  __shared__ __attribute__((aligned(16))) char lds[4 * O_STAGE];
  const int xcd = blockIdx.x & 7, idx = blockIdx.x >> 3, mt = 8 * (xcd >> 1) + (idx & 7), nt = 4 * (xcd & 1) + (idx >> 3);
  const int l = threadIdx.x & 63, w = rfl(threadIdx.x >> 6);
  const rsrc_t ra = mk_rsrc(ws + OFF_OU, OU_BYTES), rw = mk_rsrc(ws + OFF_WT + (size_t)nt * WT_TILE, WT_TILE);
  const unsigned lds0 = (unsigned)(size_t)lds, va = (mt * 64 + l) * 16, vw = l * 16;
  auto issue = [&](int st) {
    const unsigned b = lds0 + (st & 3) * O_STAGE;
#pragma unroll
    for (int i = 0; i < 4; ++i) dma16(b + (4 * w + i) * 1024, va, ra, (st * 16 + 4 * w + i) * PLANE);
#pragma unroll
    for (int i = 0; i < 5; ++i) dma16(b + O_W + (5 * w + i) * 1024, vw, rw, st * 20480 + (5 * w + i) * 1024);
  };
  issue(0); issue(1); issue(2);
  const int g = l >> 4, i16 = l & 15;
  const unsigned aa = g * 1024 + (16 * w + i16) * 16, aw = O_W + g * 1280 + i16 * 16;
  f32x4 c[5] = {};
#pragma unroll
  for (int st = 0; st < 12; ++st) {
    if (st < 10) wait_vm<18>(); else if (st == 10) wait_vm<9>(); else wait_vm<0>();
    bar_lgkm();
    if (st == 0) STAMP(1);
    if (st == 3) STAMP(2);
    if (st + 3 < 12) issue(st + 3);
    const lptr b = (lptr)lds + (st & 3) * O_STAGE;
#pragma unroll
    for (int ks = 0; ks < (st == 11 ? 3 : 4); ++ks) {
      const bf16x8 tf = ld128(b + aa + ks * 4096);
#pragma unroll
      for (int j = 0; j < 5; ++j) c[j] = __builtin_amdgcn_mfma_f32_16x16x32_bf16(ld128(b + aw + ks * 5120 + j * 256), tf, c[j], 0, 0, 0);
    }
  }
  STAMP(3);
  const int slot = 16 * w + i16, tok = mt * 64 + (slot & 32) + pi32(slot & 31);
  const rsrc_t ro = mk_rsrc(out, S * D * 2);
  const unsigned oo = tok * 1280 + nt * 160 + g * 40;
  const u32x4 t0 = {pk2(c[0][0], c[0][1]), pk2(c[0][2], c[0][3]), pk2(c[1][0], c[1][1]), pk2(c[1][2], c[1][3])};
  const u32x4 t1 = {pk2(c[2][0], c[2][1]), pk2(c[2][2], c[2][3]), pk2(c[3][0], c[3][1]), pk2(c[3][2], c[3][3])};
  const u32x2 t2 = {pk2(c[4][0], c[4][1]), pk2(c[4][2], c[4][3])};
  st128(ro, oo, t0); st128(ro, oo + 16, t1);
  __builtin_amdgcn_raw_buffer_store_b64(t2, ro, oo + 32, 0, 0);
}

extern "C" void kernel_launch(void* const* d_in, const int* in_sizes, int n_in, void* d_out, int out_size, void* d_ws, size_t ws_size, hipStream_t stream) {
  char* ws = (char*)d_ws;
  k_proj<<<256, 512, 0, stream>>>(d_in[0], d_in[1], d_in[4], d_in[8], d_in[2], d_in[7], ws);
  k_mix<<<256, 768, 0, stream>>>(d_in[0], d_in[4], d_in[5], d_in[6], ws);
  k_out<<<256, 256, 0, stream>>>(ws, d_out);
}
```

```cpp
#include <hip/hip_runtime.h>
#include <hip/hip_bf16.h>

typedef __attribute__((ext_vector_type(8))) __bf16 bf16x8;
typedef __attribute__((ext_vector_type(2))) __bf16 bf16x2;
typedef __attribute__((ext_vector_type(4))) short s16x4;
typedef __attribute__((ext_vector_type(8))) short s16x8;
typedef __attribute__((ext_vector_type(2))) float f32x2;
typedef __attribute__((ext_vector_type(4))) float f32x4;
typedef __attribute__((ext_vector_type(16))) float f32x16;
typedef __attribute__((ext_vector_type(2))) unsigned u32x2;
typedef __attribute__((ext_vector_type(4))) unsigned u32x4;
typedef __amdgpu_buffer_rsrc_t rsrc_t;
extern "C" __device__ unsigned long long dispatch_id() __asm("llvm.amdgcn.dispatch.id");
#define LAS __attribute__((address_space(3)))
#define GAS __attribute__((address_space(1)))
#define DI __device__ __forceinline__

constexpr int S = 2048, D = 640, FH = 864;
constexpr unsigned PLANE = S * 16;
constexpr unsigned QKV_BYTES = 240 * PLANE;
constexpr unsigned ZROW = FH * 16;
constexpr unsigned Z_BYTES = 256 * ZROW;
constexpr unsigned OU_BYTES = 188 * PLANE;
constexpr unsigned WT_TILE = 188 * 1280;
constexpr unsigned WT_BYTES = 8 * WT_TILE;
constexpr unsigned OFF_QKV = 0;
constexpr unsigned OFF_Z2R = OFF_QKV + QKV_BYTES;
constexpr unsigned OFF_Z2N = OFF_Z2R + Z_BYTES;
constexpr unsigned OFF_OU = OFF_Z2N + Z_BYTES;
constexpr unsigned OFF_WT = OFF_OU + OU_BYTES;
constexpr unsigned OFF_GATE = OFF_WT + WT_BYTES;
constexpr unsigned OFF_FLAG = OFF_GATE + S * 4;
constexpr unsigned FL_P = 1024, FL_L = FL_P + 8192, FL_M = FL_L + 2048, FL_BYTES = FL_M + 2048;

DI int pi32(int r) { return (r & 0x13) | ((r & 4) << 1) | ((r & 8) >> 1); }
DI rsrc_t mk_rsrc(const void* p, unsigned bytes) { return __builtin_amdgcn_make_buffer_rsrc((void*)p, (short)0, (int)bytes, 0x00020000); }
DI unsigned rfl(unsigned v) { return __builtin_amdgcn_readfirstlane(v); }
DI void dma16(unsigned m0v, unsigned voff, rsrc_t srd, unsigned soff) {
  asm volatile("s_mov_b32 m0, %0\n\ts_nop 0\n\tbuffer_load_dwordx4 %1, %2, %3 offen lds" ::"s"(m0v), "v"(voff), "s"(srd), "s"(soff) : "memory");
}
DI void dma16_sc1(unsigned m0v, unsigned voff, rsrc_t srd, unsigned soff) {
  asm volatile("s_mov_b32 m0, %0\n\ts_nop 0\n\tbuffer_load_dwordx4 %1, %2, %3 offen sc1 lds" ::"s"(m0v), "v"(voff), "s"(srd), "s"(soff) : "memory");
}
template <int N> DI void wait_vm() { asm volatile("s_waitcnt vmcnt(%0)" ::"i"(N) : "memory"); }
DI void bar() { asm volatile("s_barrier" ::: "memory"); }
DI void bar_lgkm() { asm volatile("s_waitcnt lgkmcnt(0)\n\ts_barrier" ::: "memory"); }
DI void bar_lgkm_c() { asm volatile("" ::: "memory"); __builtin_amdgcn_s_waitcnt(0xC07F); asm volatile("s_barrier" ::: "memory"); }
#define SB() __builtin_amdgcn_sched_barrier(0)
typedef const LAS char* lptr;
DI bf16x8 ld128(lptr p) { return *(const LAS bf16x8*)p; }
DI s16x4 ld64(lptr p) { return *(const LAS s16x4*)p; }
DI s16x4 ldtr(lptr p) { return __builtin_amdgcn_ds_read_tr16_b64_v4i16((LAS s16x4*)p); }
DI bf16x8 cat8(s16x4 a, s16x4 b) { return __builtin_bit_cast(bf16x8, (s16x8)__builtin_shufflevector(a, b, 0, 1, 2, 3, 4, 5, 6, 7)); }
DI bf16x8 ldtr8(lptr p, int off2) { return cat8(ldtr(p), ldtr(p + off2)); }
DI f32x16 mfma16(bf16x8 a, bf16x8 b, f32x16 c) { return __builtin_amdgcn_mfma_f32_32x32x16_bf16(a, b, c, 0, 0, 0); }
DI f32x16 mfma8(s16x4 a, s16x4 b, f32x16 c) { return __builtin_amdgcn_mfma_f32_32x32x8bf16_1k(a, b, c, 0, 0, 0); }
DI unsigned pk2(float a, float b) { f32x2 v = {a, b}; return __builtin_bit_cast(unsigned, __builtin_convertvector(v, bf16x2)); }
DI u32x4 pk8(const f32x16& a, int o) { u32x4 r = {pk2(a[o], a[o + 1]), pk2(a[o + 2], a[o + 3]), pk2(a[o + 4], a[o + 5]), pk2(a[o + 6], a[o + 7])}; return r; }
DI void gelu16(f32x16& a) {
  const float k1 = -2.0f * 0.7978845608028654f * 1.4426950408889634f, k2 = k1 * 0.044715f;
#pragma unroll
  for (int i = 0; i < 16; i += 2) {
    const f32x2 v = {a[i], a[i + 1]};
    const f32x2 t = v * __builtin_elementwise_fma(v * v, f32x2{k2, k2}, f32x2{k1, k1});
    const f32x2 d = f32x2{__builtin_amdgcn_exp2f(t[0]), __builtin_amdgcn_exp2f(t[1])} + 1.0f;
    const f32x2 y = v * f32x2{__builtin_amdgcn_rcpf(d[0]), __builtin_amdgcn_rcpf(d[1])};
    a[i] = y[0]; a[i + 1] = y[1];
  }
}
DI void st128(rsrc_t r, unsigned voff, u32x4 v) { __builtin_amdgcn_raw_buffer_store_b128(v, r, voff, 0, 16); }

constexpr int P_STAGE = 40960, P_WQ = 16384, P_WZ = 32768;

template <bool GATE>
DI void proj_loader(char* lds, const void* x, const void* wqkv, const void* win, const void* gw, int lw, int panel, int ct) {
  const int l = threadIdx.x & 63;
  const rsrc_t rx = mk_rsrc(x, S * D * 2), rq = mk_rsrc(wqkv, D * 1920 * 2);
  const rsrc_t rz = GATE ? mk_rsrc(gw, D * 2) : mk_rsrc(win, D * 1728 * 2);
  const unsigned lds0 = (unsigned)(size_t)lds;
  unsigned vx[4], vq[2], vz[2];
#pragma unroll
  for (int i = 0; i < 4; ++i) vx[i] = (panel * 128 + 32 * lw + 8 * i + (l >> 3)) * 1280 + (((l & 7) ^ (((l >> 4) & 1) | (i << 1))) << 4);
#pragma unroll
  for (int i = 0; i < 2; ++i) {
    vq[i] = (16 * lw + 8 * i + (l >> 4)) * 3840 + ct * 256 + (((l & 15) ^ (((l >> 4) | (i << 2)) << 1)) << 4);
    vz[i] = GATE ? (l & 7) << 4 : (16 * lw + 8 * i + (l >> 3)) * 3456 + (832 + 64 * ct) * 2 + (((l & 7) ^ ((((l >> 4) & 1) | (i << 1)) << 1)) << 4);
  }
  auto issue = [&](int st, int from = 0) {
    const unsigned b = lds0 + (st & 3) * P_STAGE;
#pragma unroll
    for (int i = 0; i < 4; ++i) if (i >= from) dma16(b + lw * 4096 + i * 1024, vx[i], rx, st * 128);
#pragma unroll
    for (int i = 0; i < 4; ++i) dma16(b + P_WQ + lw * 4096 + i * 1024, vq[i >> 1], rq, (st * 64 + 4 * (i & 1)) * 3840);
#pragma unroll
    for (int i = 0; i < 2; ++i) dma16(b + P_WZ + lw * 2048 + i * 1024, vz[i], rz, GATE ? st * 128 : st * 64 * 3456);
  };
  unsigned pf0 = 0;
  issue(0);
  {
    const int bi = panel * 15 + ct, i = lw * 56 + min(l, 55);
    const int ix = min(bi * 77 + i, 2048 * 9 - 1), iq = bi * 72 + i - 77, iz = bi * 72 + min(i - 149, 71);
    const char* p = i < 77 ? (const char*)x + (ix / 9) * 1280 + (1 + ix % 9) * 128 : i < 149 ? (const char*)wqkv + (64 * 30 + iq) * 128 : (const char*)win + iz * 128;
    asm volatile("global_load_dword %0, %1, off" : "=v"(pf0) : "v"(p) : "memory");
  }
#pragma unroll
  for (int i = 0; i < 2; ++i) dma16(lds0 + P_STAGE + lw * 4096 + i * 1024, vx[i], rx, 128);
  wait_vm<3>(); bar();
  issue(1, 2); issue(2);
#pragma unroll 1
  for (int st = 0; st < 7; ++st) { wait_vm<10>(); bar(); issue(st + 3); }
  asm volatile("" :: "v"(pf0));
}

#define PROJ_LOOP0 \
  "ds_read_b64_tr_b16 v[144:145], v162 offset:0\n" \
  "ds_read_b64_tr_b16 v[146:147], v162 offset:1024\n" \
  "ds_read_b128 v[112:115], v160 offset:0\n" \
  "ds_read_b128 v[116:119], v160 offset:512\n" \
  "ds_read_b64_tr_b16 v[148:149], v163 offset:0\n" \
  "ds_read_b64_tr_b16 v[150:151], v163 offset:1024\n" \
  "ds_read_b128 v[120:123], v160 offset:4096\n" \
  "ds_read_b128 v[124:127], v160 offset:4608\n" \
  "ds_read_b64_tr_b16 v[152:153], v164 offset:0\n" \
  "ds_read_b64_tr_b16 v[154:155], v164 offset:1024\n" \
  "v_mov_b64 v[16:17], 0\n" \
  "v_mov_b64 v[18:19], 0\n" \
  "v_mov_b64 v[20:21], 0\n" \
  "v_mov_b64 v[22:23], 0\n" \
  "v_mov_b64 v[24:25], 0\n" \
  "v_mov_b64 v[26:27], 0\n" \
  "v_mov_b64 v[28:29], 0\n" \
  "v_mov_b64 v[30:31], 0\n" \
  "v_mov_b64 v[32:33], 0\n" \
  "v_mov_b64 v[34:35], 0\n" \
  "v_mov_b64 v[36:37], 0\n" \
  "v_mov_b64 v[38:39], 0\n" \
  "v_mov_b64 v[40:41], 0\n" \
  "v_mov_b64 v[42:43], 0\n" \
  "v_mov_b64 v[44:45], 0\n" \
  "v_mov_b64 v[46:47], 0\n" \
  "v_mov_b64 v[48:49], 0\n" \
  "v_mov_b64 v[50:51], 0\n" \
  "v_mov_b64 v[52:53], 0\n" \
  "v_mov_b64 v[54:55], 0\n" \
  "v_mov_b64 v[56:57], 0\n" \
  "v_mov_b64 v[58:59], 0\n" \
  "v_mov_b64 v[60:61], 0\n" \
  "v_mov_b64 v[62:63], 0\n" \
  "v_mov_b64 v[64:65], 0\n" \
  "v_mov_b64 v[66:67], 0\n" \
  "v_mov_b64 v[68:69], 0\n" \
  "v_mov_b64 v[70:71], 0\n" \
  "v_mov_b64 v[72:73], 0\n" \
  "v_mov_b64 v[74:75], 0\n" \
  "v_mov_b64 v[76:77], 0\n" \
  "v_mov_b64 v[78:79], 0\n" \
  "v_mov_b64 v[80:81], 0\n" \
  "v_mov_b64 v[82:83], 0\n" \
  "v_mov_b64 v[84:85], 0\n" \
  "v_mov_b64 v[86:87], 0\n" \
  "v_mov_b64 v[88:89], 0\n" \
  "v_mov_b64 v[90:91], 0\n" \
  "v_mov_b64 v[92:93], 0\n" \
  "v_mov_b64 v[94:95], 0\n" \
  "v_mov_b64 v[96:97], 0\n" \
  "v_mov_b64 v[98:99], 0\n" \
  "v_mov_b64 v[100:101], 0\n" \
  "v_mov_b64 v[102:103], 0\n" \
  "v_mov_b64 v[104:105], 0\n" \
  "v_mov_b64 v[106:107], 0\n" \
  "v_mov_b64 v[108:109], 0\n" \
  "v_mov_b64 v[110:111], 0\n" \
  "s_mov_b32 %[st], 0\n" \
  "1:\n" \
  "s_add_u32 %[st], %[st], 1\n" \
  "s_and_b32 %[dl], %[st], 3\n" \
  "s_cmp_eq_u32 %[dl], 0\n" \
  "s_cselect_b32 %[dl], %[wr], %[fw]\n" \
  "s_waitcnt lgkmcnt(7)\n" \
  "v_mfma_f32_16x16x32_bf16 v[16:19], v[144:147], v[112:115], v[16:19]\n" \
  "ds_read_b64_tr_b16 v[156:157], v165 offset:0\n" \
  "ds_read_b64_tr_b16 v[158:159], v165 offset:1024\n" \
  "s_waitcnt lgkmcnt(8)\n" \
  "v_mfma_f32_16x16x32_bf16 v[20:23], v[144:147], v[116:119], v[20:23]\n" \
  "s_waitcnt lgkmcnt(5)\n" \
  "v_mfma_f32_16x16x32_bf16 v[24:27], v[144:147], v[120:123], v[24:27]\n" \
  "ds_read_b128 v[128:131], v161 offset:0\n" \
  "s_waitcnt lgkmcnt(5)\n" \
  "v_mfma_f32_16x16x32_bf16 v[28:31], v[144:147], v[124:127], v[28:31]\n" \
  "ds_read_b128 v[132:135], v161 offset:512\n" \
  "v_mfma_f32_16x16x32_bf16 v[32:35], v[148:151], v[112:115], v[32:35]\n" \
  "ds_read_b64_tr_b16 v[144:145], v166 offset:0\n" \
  "ds_read_b64_tr_b16 v[146:147], v166 offset:512\n" \
  "v_mfma_f32_16x16x32_bf16 v[36:39], v[148:151], v[116:119], v[36:39]\n" \
  "v_mfma_f32_16x16x32_bf16 v[40:43], v[148:151], v[120:123], v[40:43]\n" \
  "ds_read_b128 v[136:139], v161 offset:4096\n" \
  "v_mfma_f32_16x16x32_bf16 v[44:47], v[148:151], v[124:127], v[44:47]\n" \
  "ds_read_b128 v[140:143], v161 offset:4608\n" \
  "s_waitcnt lgkmcnt(8)\n" \
  "v_mfma_f32_16x16x32_bf16 v[48:51], v[152:155], v[112:115], v[48:51]\n" \
  "ds_read_b64_tr_b16 v[148:149], v167 offset:0\n" \
  "ds_read_b64_tr_b16 v[150:151], v167 offset:512\n" \
  "v_mfma_f32_16x16x32_bf16 v[52:55], v[152:155], v[116:119], v[52:55]\n" \
  "v_mfma_f32_16x16x32_bf16 v[56:59], v[152:155], v[120:123], v[56:59]\n" \
  "v_mfma_f32_16x16x32_bf16 v[60:63], v[152:155], v[124:127], v[60:63]\n" \
  "s_waitcnt lgkmcnt(8)\n" \
  "v_mfma_f32_16x16x32_bf16 v[64:67], v[156:159], v[112:115], v[64:67]\n" \
  "ds_read_b64_tr_b16 v[152:153], v162 offset:8192\n" \
  "ds_read_b64_tr_b16 v[154:155], v162 offset:9216\n" \
  "v_mfma_f32_16x16x32_bf16 v[68:71], v[156:159], v[116:119], v[68:71]\n" \
  "v_mfma_f32_16x16x32_bf16 v[72:75], v[156:159], v[120:123], v[72:75]\n" \
  "v_mfma_f32_16x16x32_bf16 v[76:79], v[156:159], v[124:127], v[76:79]\n" \
  "s_waitcnt lgkmcnt(6)\n" \
  "v_mfma_f32_16x16x32_bf16 v[80:83], v[112:115], v[144:147], v[80:83]\n" \
  "ds_read_b64_tr_b16 v[156:157], v163 offset:8192\n" \
  "ds_read_b64_tr_b16 v[158:159], v163 offset:9216\n" \
  "v_mfma_f32_16x16x32_bf16 v[84:87], v[116:119], v[144:147], v[84:87]\n" \
  "v_mfma_f32_16x16x32_bf16 v[88:91], v[120:123], v[144:147], v[88:91]\n" \
  "v_mfma_f32_16x16x32_bf16 v[92:95], v[124:127], v[144:147], v[92:95]\n" \
  "s_waitcnt lgkmcnt(4)\n" \
  "v_mfma_f32_16x16x32_bf16 v[96:99], v[112:115], v[148:151], v[96:99]\n" \
  "ds_read_b64_tr_b16 v[144:145], v164 offset:8192\n" \
  "ds_read_b64_tr_b16 v[146:147], v164 offset:9216\n" \
  "v_mfma_f32_16x16x32_bf16 v[100:103], v[116:119], v[148:151], v[100:103]\n" \
  "v_mfma_f32_16x16x32_bf16 v[104:107], v[120:123], v[148:151], v[104:107]\n" \
  "v_mfma_f32_16x16x32_bf16 v[108:111], v[124:127], v[148:151], v[108:111]\n" \
  "s_waitcnt lgkmcnt(4)\n" \
  "v_mfma_f32_16x16x32_bf16 v[16:19], v[152:155], v[128:131], v[16:19]\n" \
  "ds_read_b64_tr_b16 v[148:149], v165 offset:8192\n" \
  "ds_read_b64_tr_b16 v[150:151], v165 offset:9216\n" \
  "v_mfma_f32_16x16x32_bf16 v[20:23], v[152:155], v[132:135], v[20:23]\n" \
  "v_add_u32 v160, %[dl], v160\n" \
  "v_add_u32 v161, %[dl], v161\n" \
  "v_mfma_f32_16x16x32_bf16 v[24:27], v[152:155], v[136:139], v[24:27]\n" \
  "v_add_u32 v162, %[dl], v162\n" \
  "v_add_u32 v163, %[dl], v163\n" \
  "v_mfma_f32_16x16x32_bf16 v[28:31], v[152:155], v[140:143], v[28:31]\n" \
  "v_add_u32 v164, %[dl], v164\n" \
  "v_add_u32 v165, %[dl], v165\n" \
  "s_waitcnt lgkmcnt(4)\n" \
  "v_mfma_f32_16x16x32_bf16 v[32:35], v[156:159], v[128:131], v[32:35]\n" \
  "ds_read_b64_tr_b16 v[152:153], v166 offset:4096\n" \
  "ds_read_b64_tr_b16 v[154:155], v166 offset:4608\n" \
  "v_mfma_f32_16x16x32_bf16 v[36:39], v[156:159], v[132:135], v[36:39]\n" \
  "v_mfma_f32_16x16x32_bf16 v[40:43], v[156:159], v[136:139], v[40:43]\n" \
  "v_mfma_f32_16x16x32_bf16 v[44:47], v[156:159], v[140:143], v[44:47]\n" \
  "s_waitcnt lgkmcnt(4)\n" \
  "v_mfma_f32_16x16x32_bf16 v[48:51], v[144:147], v[128:131], v[48:51]\n" \
  "ds_read_b64_tr_b16 v[156:157], v167 offset:4096\n" \
  "ds_read_b64_tr_b16 v[158:159], v167 offset:4608\n" \
  "v_mfma_f32_16x16x32_bf16 v[52:55], v[144:147], v[132:135], v[52:55]\n" \
  "v_add_u32 v166, %[dl], v166\n" \
  "v_add_u32 v167, %[dl], v167\n" \
  "v_mfma_f32_16x16x32_bf16 v[56:59], v[144:147], v[136:139], v[56:59]\n" \
  "v_mfma_f32_16x16x32_bf16 v[60:63], v[144:147], v[140:143], v[60:63]\n" \
  "s_waitcnt lgkmcnt(0)\n" \
  "s_barrier\n" \
  "v_mfma_f32_16x16x32_bf16 v[64:67], v[148:151], v[128:131], v[64:67]\n" \
  "ds_read_b64_tr_b16 v[144:145], v162 offset:0\n" \
  "ds_read_b64_tr_b16 v[146:147], v162 offset:1024\n" \
  "v_mfma_f32_16x16x32_bf16 v[68:71], v[148:151], v[132:135], v[68:71]\n" \
  "ds_read_b128 v[112:115], v160 offset:0\n" \
  "v_mfma_f32_16x16x32_bf16 v[72:75], v[148:151], v[136:139], v[72:75]\n" \
  "ds_read_b128 v[116:119], v160 offset:512\n" \
  "v_mfma_f32_16x16x32_bf16 v[76:79], v[148:151], v[140:143], v[76:79]\n" \
  "v_mfma_f32_16x16x32_bf16 v[80:83], v[128:131], v[152:155], v[80:83]\n" \
  "ds_read_b64_tr_b16 v[148:149], v163 offset:0\n" \
  "ds_read_b64_tr_b16 v[150:151], v163 offset:1024\n" \
  "v_mfma_f32_16x16x32_bf16 v[84:87], v[132:135], v[152:155], v[84:87]\n" \
  "ds_read_b128 v[120:123], v160 offset:4096\n" \
  "v_mfma_f32_16x16x32_bf16 v[88:91], v[136:139], v[152:155], v[88:91]\n" \
  "ds_read_b128 v[124:127], v160 offset:4608\n" \
  "v_mfma_f32_16x16x32_bf16 v[92:95], v[140:143], v[152:155], v[92:95]\n" \
  "v_mfma_f32_16x16x32_bf16 v[96:99], v[128:131], v[156:159], v[96:99]\n" \
  "ds_read_b64_tr_b16 v[152:153], v164 offset:0\n" \
  "ds_read_b64_tr_b16 v[154:155], v164 offset:1024\n" \
  "v_mfma_f32_16x16x32_bf16 v[100:103], v[132:135], v[156:159], v[100:103]\n" \
  "v_mfma_f32_16x16x32_bf16 v[104:107], v[136:139], v[156:159], v[104:107]\n" \
  "v_mfma_f32_16x16x32_bf16 v[108:111], v[140:143], v[156:159], v[108:111]\n" \
  "s_cmp_lg_u32 %[st], 10\n" \
  "s_cbranch_scc1 1b\n" \
  "s_waitcnt lgkmcnt(0)\n" \
  "s_nop 7\n" \
  ""
#define PROJ_LOOP1 \
  "ds_read_b64_tr_b16 v[144:145], v162 offset:0\n" \
  "ds_read_b64_tr_b16 v[146:147], v162 offset:1024\n" \
  "ds_read_b128 v[112:115], v160 offset:0\n" \
  "ds_read_b128 v[116:119], v160 offset:512\n" \
  "ds_read_b64_tr_b16 v[148:149], v163 offset:0\n" \
  "ds_read_b64_tr_b16 v[150:151], v163 offset:1024\n" \
  "ds_read_b128 v[120:123], v160 offset:4096\n" \
  "ds_read_b128 v[124:127], v160 offset:4608\n" \
  "ds_read_b64_tr_b16 v[152:153], v164 offset:0\n" \
  "ds_read_b64_tr_b16 v[154:155], v164 offset:1024\n" \
  "v_mov_b64 v[16:17], 0\n" \
  "v_mov_b64 v[18:19], 0\n" \
  "v_mov_b64 v[20:21], 0\n" \
  "v_mov_b64 v[22:23], 0\n" \
  "v_mov_b64 v[24:25], 0\n" \
  "v_mov_b64 v[26:27], 0\n" \
  "v_mov_b64 v[28:29], 0\n" \
  "v_mov_b64 v[30:31], 0\n" \
  "v_mov_b64 v[32:33], 0\n" \
  "v_mov_b64 v[34:35], 0\n" \
  "v_mov_b64 v[36:37], 0\n" \
  "v_mov_b64 v[38:39], 0\n" \
  "v_mov_b64 v[40:41], 0\n" \
  "v_mov_b64 v[42:43], 0\n" \
  "v_mov_b64 v[44:45], 0\n" \
  "v_mov_b64 v[46:47], 0\n" \
  "v_mov_b64 v[48:49], 0\n" \
  "v_mov_b64 v[50:51], 0\n" \
  "v_mov_b64 v[52:53], 0\n" \
  "v_mov_b64 v[54:55], 0\n" \
  "v_mov_b64 v[56:57], 0\n" \
  "v_mov_b64 v[58:59], 0\n" \
  "v_mov_b64 v[60:61], 0\n" \
  "v_mov_b64 v[62:63], 0\n" \
  "v_mov_b64 v[64:65], 0\n" \
  "v_mov_b64 v[66:67], 0\n" \
  "v_mov_b64 v[68:69], 0\n" \
  "v_mov_b64 v[70:71], 0\n" \
  "v_mov_b64 v[72:73], 0\n" \
  "v_mov_b64 v[74:75], 0\n" \
  "v_mov_b64 v[76:77], 0\n" \
  "v_mov_b64 v[78:79], 0\n" \
  "v_mov_b64 v[80:81], 0\n" \
  "v_mov_b64 v[82:83], 0\n" \
  "v_mov_b64 v[84:85], 0\n" \
  "v_mov_b64 v[86:87], 0\n" \
  "v_mov_b64 v[88:89], 0\n" \
  "v_mov_b64 v[90:91], 0\n" \
  "v_mov_b64 v[92:93], 0\n" \
  "v_mov_b64 v[94:95], 0\n" \
  "v_mov_b64 v[96:97], 0\n" \
  "v_mov_b64 v[98:99], 0\n" \
  "v_mov_b64 v[100:101], 0\n" \
  "v_mov_b64 v[102:103], 0\n" \
  "v_mov_b64 v[104:105], 0\n" \
  "v_mov_b64 v[106:107], 0\n" \
  "v_mov_b64 v[108:109], 0\n" \
  "v_mov_b64 v[110:111], 0\n" \
  "s_mov_b32 %[st], 0\n" \
  "1:\n" \
  "s_add_u32 %[st], %[st], 1\n" \
  "s_and_b32 %[dl], %[st], 3\n" \
  "s_cmp_eq_u32 %[dl], 0\n" \
  "s_cselect_b32 %[dl], %[wr], %[fw]\n" \
  "s_waitcnt lgkmcnt(7)\n" \
  "v_mfma_f32_16x16x32_bf16 v[16:19], v[144:147], v[112:115], v[16:19]\n" \
  "ds_read_b64_tr_b16 v[156:157], v165 offset:0\n" \
  "ds_read_b64_tr_b16 v[158:159], v165 offset:1024\n" \
  "s_waitcnt lgkmcnt(8)\n" \
  "v_mfma_f32_16x16x32_bf16 v[20:23], v[144:147], v[116:119], v[20:23]\n" \
  "s_waitcnt lgkmcnt(5)\n" \
  "v_mfma_f32_16x16x32_bf16 v[24:27], v[144:147], v[120:123], v[24:27]\n" \
  "ds_read_b128 v[128:131], v161 offset:0\n" \
  "s_waitcnt lgkmcnt(5)\n" \
  "v_mfma_f32_16x16x32_bf16 v[28:31], v[144:147], v[124:127], v[28:31]\n" \
  "ds_read_b128 v[132:135], v161 offset:512\n" \
  "v_mfma_f32_16x16x32_bf16 v[32:35], v[148:151], v[112:115], v[32:35]\n" \
  "ds_read_b128 v[144:147], v166 offset:0\n" \
  "v_mfma_f32_16x16x32_bf16 v[36:39], v[148:151], v[116:119], v[36:39]\n" \
  "v_mfma_f32_16x16x32_bf16 v[40:43], v[148:151], v[120:123], v[40:43]\n" \
  "ds_read_b128 v[136:139], v161 offset:4096\n" \
  "v_mfma_f32_16x16x32_bf16 v[44:47], v[148:151], v[124:127], v[44:47]\n" \
  "ds_read_b128 v[140:143], v161 offset:4608\n" \
  "s_waitcnt lgkmcnt(7)\n" \
  "v_mfma_f32_16x16x32_bf16 v[48:51], v[152:155], v[112:115], v[48:51]\n" \
  "ds_read_b128 v[148:151], v166 offset:0\n" \
  "v_mfma_f32_16x16x32_bf16 v[52:55], v[152:155], v[116:119], v[52:55]\n" \
  "v_mfma_f32_16x16x32_bf16 v[56:59], v[152:155], v[120:123], v[56:59]\n" \
  "v_mfma_f32_16x16x32_bf16 v[60:63], v[152:155], v[124:127], v[60:63]\n" \
  "s_waitcnt lgkmcnt(6)\n" \
  "v_mfma_f32_16x16x32_bf16 v[64:67], v[156:159], v[112:115], v[64:67]\n" \
  "ds_read_b64_tr_b16 v[152:153], v162 offset:8192\n" \
  "ds_read_b64_tr_b16 v[154:155], v162 offset:9216\n" \
  "v_mfma_f32_16x16x32_bf16 v[68:71], v[156:159], v[116:119], v[68:71]\n" \
  "v_mfma_f32_16x16x32_bf16 v[72:75], v[156:159], v[120:123], v[72:75]\n" \
  "v_mfma_f32_16x16x32_bf16 v[76:79], v[156:159], v[124:127], v[76:79]\n" \
  "s_waitcnt lgkmcnt(5)\n" \
  "v_mfma_f32_16x16x32_bf16 v[80:83], v[112:115], v[144:147], v[80:83]\n" \
  "ds_read_b64_tr_b16 v[156:157], v163 offset:8192\n" \
  "ds_read_b64_tr_b16 v[158:159], v163 offset:9216\n" \
  "v_mfma_f32_16x16x32_bf16 v[84:87], v[116:119], v[144:147], v[84:87]\n" \
  "v_mfma_f32_16x16x32_bf16 v[88:91], v[120:123], v[144:147], v[88:91]\n" \
  "v_mfma_f32_16x16x32_bf16 v[92:95], v[124:127], v[144:147], v[92:95]\n" \
  "s_waitcnt lgkmcnt(4)\n" \
  "v_mfma_f32_16x16x32_bf16 v[96:99], v[112:115], v[148:151], v[96:99]\n" \
  "ds_read_b64_tr_b16 v[144:145], v164 offset:8192\n" \
  "ds_read_b64_tr_b16 v[146:147], v164 offset:9216\n" \
  "v_mfma_f32_16x16x32_bf16 v[100:103], v[116:119], v[148:151], v[100:103]\n" \
  "v_mfma_f32_16x16x32_bf16 v[104:107], v[120:123], v[148:151], v[104:107]\n" \
  "v_mfma_f32_16x16x32_bf16 v[108:111], v[124:127], v[148:151], v[108:111]\n" \
  "s_waitcnt lgkmcnt(4)\n" \
  "v_mfma_f32_16x16x32_bf16 v[16:19], v[152:155], v[128:131], v[16:19]\n" \
  "ds_read_b64_tr_b16 v[148:149], v165 offset:8192\n" \
  "ds_read_b64_tr_b16 v[150:151], v165 offset:9216\n" \
  "v_mfma_f32_16x16x32_bf16 v[20:23], v[152:155], v[132:135], v[20:23]\n" \
  "v_add_u32 v160, %[dl], v160\n" \
  "v_add_u32 v161, %[dl], v161\n" \
  "v_mfma_f32_16x16x32_bf16 v[24:27], v[152:155], v[136:139], v[24:27]\n" \
  "v_add_u32 v162, %[dl], v162\n" \
  "v_add_u32 v163, %[dl], v163\n" \
  "v_mfma_f32_16x16x32_bf16 v[28:31], v[152:155], v[140:143], v[28:31]\n" \
  "v_add_u32 v164, %[dl], v164\n" \
  "v_add_u32 v165, %[dl], v165\n" \
  "s_waitcnt lgkmcnt(4)\n" \
  "v_mfma_f32_16x16x32_bf16 v[32:35], v[156:159], v[128:131], v[32:35]\n" \
  "ds_read_b128 v[152:155], v166 offset:64\n" \
  "v_mfma_f32_16x16x32_bf16 v[36:39], v[156:159], v[132:135], v[36:39]\n" \
  "v_mfma_f32_16x16x32_bf16 v[40:43], v[156:159], v[136:139], v[40:43]\n" \
  "v_mfma_f32_16x16x32_bf16 v[44:47], v[156:159], v[140:143], v[44:47]\n" \
  "s_waitcnt lgkmcnt(3)\n" \
  "v_mfma_f32_16x16x32_bf16 v[48:51], v[144:147], v[128:131], v[48:51]\n" \
  "ds_read_b128 v[156:159], v166 offset:64\n" \
  "v_mfma_f32_16x16x32_bf16 v[52:55], v[144:147], v[132:135], v[52:55]\n" \
  "v_add_u32 v166, %[dl], v166\n" \
  "v_add_u32 v167, %[dl], v167\n" \
  "v_mfma_f32_16x16x32_bf16 v[56:59], v[144:147], v[136:139], v[56:59]\n" \
  "v_mfma_f32_16x16x32_bf16 v[60:63], v[144:147], v[140:143], v[60:63]\n" \
  "s_waitcnt lgkmcnt(0)\n" \
  "s_barrier\n" \
  "v_mfma_f32_16x16x32_bf16 v[64:67], v[148:151], v[128:131], v[64:67]\n" \
  "ds_read_b64_tr_b16 v[144:145], v162 offset:0\n" \
  "ds_read_b64_tr_b16 v[146:147], v162 offset:1024\n" \
  "v_mfma_f32_16x16x32_bf16 v[68:71], v[148:151], v[132:135], v[68:71]\n" \
  "ds_read_b128 v[112:115], v160 offset:0\n" \
  "v_mfma_f32_16x16x32_bf16 v[72:75], v[148:151], v[136:139], v[72:75]\n" \
  "ds_read_b128 v[116:119], v160 offset:512\n" \
  "v_mfma_f32_16x16x32_bf16 v[76:79], v[148:151], v[140:143], v[76:79]\n" \
  "v_mfma_f32_16x16x32_bf16 v[80:83], v[128:131], v[152:155], v[80:83]\n" \
  "ds_read_b64_tr_b16 v[148:149], v163 offset:0\n" \
  "ds_read_b64_tr_b16 v[150:151], v163 offset:1024\n" \
  "v_mfma_f32_16x16x32_bf16 v[84:87], v[132:135], v[152:155], v[84:87]\n" \
  "ds_read_b128 v[120:123], v160 offset:4096\n" \
  "v_mfma_f32_16x16x32_bf16 v[88:91], v[136:139], v[152:155], v[88:91]\n" \
  "ds_read_b128 v[124:127], v160 offset:4608\n" \
  "v_mfma_f32_16x16x32_bf16 v[92:95], v[140:143], v[152:155], v[92:95]\n" \
  "v_mfma_f32_16x16x32_bf16 v[96:99], v[128:131], v[156:159], v[96:99]\n" \
  "ds_read_b64_tr_b16 v[152:153], v164 offset:0\n" \
  "ds_read_b64_tr_b16 v[154:155], v164 offset:1024\n" \
  "v_mfma_f32_16x16x32_bf16 v[100:103], v[132:135], v[156:159], v[100:103]\n" \
  "v_mfma_f32_16x16x32_bf16 v[104:107], v[136:139], v[156:159], v[104:107]\n" \
  "v_mfma_f32_16x16x32_bf16 v[108:111], v[140:143], v[156:159], v[108:111]\n" \
  "s_cmp_lg_u32 %[st], 10\n" \
  "s_cbranch_scc1 1b\n" \
  "s_waitcnt lgkmcnt(0)\n" \
  "s_nop 7\n" \
  ""
#define C8(b) "v" #b "0", "v" #b "1", "v" #b "2", "v" #b "3", "v" #b "4", "v" #b "5", "v" #b "6", "v" #b "7", "v" #b "8", "v" #b "9"
template <bool GATE>
DI void proj_body(char* lds, char* ws, int w, int panel, int ct) {
  const int l = threadIdx.x & 63, wm = w & 1, wn = w >> 1, i16 = l & 15, g = l >> 4, q = (l >> 2) & 3, p = l & 3;
  const unsigned lb = (unsigned)(size_t)lds;
  const int xr = 64 * wm + 8 * (i16 >> 2) + (i16 & 3);
  u32x2 ax, az; u32x4 aq;
  ax[0] = lb + xr * 128 + ((g ^ (((xr >> 1) & 1) | (((xr >> 3) & 3) << 1))) << 4); ax[1] = ax[0] ^ 64;
#pragma unroll
  for (int c = 0; c < 4; ++c) aq[c] = lb + P_WQ + (8 * g + q) * 256 + (((2 * (4 * wn + c) + (p >> 1)) ^ ((q | ((g & 1) << 2)) << 1)) << 4) + (p & 1) * 8;
#pragma unroll
  for (int c = 0; c < 2; ++c)
    az[c] = GATE ? lb + P_WZ + g * 16 : lb + P_WZ + (8 * g + q) * 128 + (((2 * (2 * wn + c) + (p >> 1)) ^ (((q >> 1) | ((g & 1) << 1)) << 1)) << 4) + (p & 1) * 8;
  asm volatile("" : "+{v[160:161]}"(ax), "+{v[162:165]}"(aq), "+{v[166:167]}"(az));
  bar();
  f32x16 c0, c1, c2, c3, z0, z1;
  unsigned st, dl;
#define PROJ_RUN(T) asm volatile(T : "={v[16:31]}"(c0), "={v[32:47]}"(c1), "={v[48:63]}"(c2), "={v[64:79]}"(c3), "={v[80:95]}"(z0), "={v[96:111]}"(z1), \
      "+{v[160:161]}"(ax), "+{v[162:165]}"(aq), "+{v[166:167]}"(az), [st] "=&s"(st), [dl] "=&s"(dl) : [fw] "s"(P_STAGE), [wr] "s"(-3 * P_STAGE) \
      : "v112", "v113", "v114", "v115", "v116", "v117", "v118", "v119", C8(12), C8(13), C8(14), C8(15), "scc", "memory")
  if (GATE) PROJ_RUN(PROJ_LOOP1); else PROJ_RUN(PROJ_LOOP0);
  const rsrc_t ro = mk_rsrc(ws + OFF_QKV, QKV_BYTES);
  if (ct < 5) { const float sc = 0.22360679774997896f * 1.4426950408889634f; c0 *= sc; c1 *= sc; c2 *= sc; c3 *= sc; }
  const unsigned po = (ct * 16 + wn * 8 + (((g & 1) << 1) | (g >> 1))) * PLANE + (panel * 128 + wm * 64 + (i16 & 7) + 16 * (i16 >> 3)) * 16;
  auto put = [&](const f32x16& a, const f32x16& b, int pl) {
#pragma unroll
    for (int tm = 0; tm < 4; ++tm) {
      const auto s0 = __builtin_amdgcn_permlane16_swap(pk2(a[4 * tm], a[4 * tm + 1]), pk2(b[4 * tm], b[4 * tm + 1]), false, false);
      const auto s1 = __builtin_amdgcn_permlane16_swap(pk2(a[4 * tm + 2], a[4 * tm + 3]), pk2(b[4 * tm + 2], b[4 * tm + 3]), false, false);
      st128(ro, po + pl * PLANE + ((tm >> 1) * 32 + (tm & 1) * 8) * 16, u32x4{s0[0], s1[0], s0[1], s1[1]});
    }
  };
  put(c0, c1, 0); put(c2, c3, 4);
  if (GATE) {
    if (wn == 0 && i16 == 0) {
      const rsrc_t rg = mk_rsrc(ws + OFF_GATE, S * 4);
#pragma unroll
      for (int k = 0; k < 4; ++k) {
        u32x4 t;
#pragma unroll
        for (int i = 0; i < 4; ++i) t[i] = __builtin_bit_cast(unsigned, __builtin_amdgcn_rcpf(1.0f + __builtin_amdgcn_exp2f(-1.4426950408889634f * z0[4 * k + i])));
        st128(rg, (panel * 128 + wm * 64 + (k >> 1) * 32 + 8 * g + (k & 1) * 4) * 4, t);
      }
    }
  } else {
    const int ft = 2 * ct + wn - 1;
    if (ft >= 0) {
      const rsrc_t rzo = mk_rsrc(ws + OFF_Z2R, Z_BYTES);
      const unsigned zo = (panel * 16 + wm * 8 + g) * ZROW + (ft * 32 + pi32(i16)) * 16;
      gelu16(z0);
      st128(rzo, zo, pk8(z0, 0)); st128(rzo, zo + 4 * ZROW, pk8(z0, 8));
      gelu16(z1);
      st128(rzo, zo + 256, pk8(z1, 0)); st128(rzo, zo + 4 * ZROW + 256, pk8(z1, 8));
    }
  }
}

DI void pack_weights(char* lds, const void* wo, const void* wout, char* ws, int pb) {
  const int tid = threadIdx.x;
  const rsrc_t r0 = mk_rsrc(wo, D * D * 2), r1 = mk_rsrc(wout, FH * D * 2), rw = mk_rsrc(ws + OFF_WT, WT_BYTES);
  u32x2 v[4][8];
#pragma unroll
  for (int it = 0; it < 4; ++it) {
    const int item = (it * 16 + pb) * 512 + tid, P = item / 160, nq = item % 160;
#pragma unroll
    for (int k = 0; k < 8; ++k) {
      const unsigned o0 = P < 80 ? (P * 8 + k) * 1280 + nq * 8 : 0xF0000000u, o1 = P >= 80 ? ((P - 80) * 8 + k) * 1280 + nq * 8 : 0xF0000000u;
      v[it][k] = __builtin_amdgcn_raw_buffer_load_b64(r0, o0, 0, 0) | __builtin_amdgcn_raw_buffer_load_b64(r1, o1, 0, 0);
    }
  }
  const int l = tid & 63;
  LAS char* tb = (LAS char*)lds + (tid >> 6) * 4096;
#pragma unroll
  for (int it = 0; it < 4; ++it) {
#pragma unroll
    for (int e = 0; e < 4; ++e) {
      const unsigned sel = (e & 1) ? 0x07060302u : 0x05040100u;
      u32x4 t;
#pragma unroll
      for (int k = 0; k < 4; ++k) t[k] = __builtin_amdgcn_perm(v[it][2 * k + 1][e >> 1], v[it][2 * k][e >> 1], sel);
      *(LAS u32x4*)(tb + l * 64 + e * 16) = t;
    }
    asm volatile("s_waitcnt lgkmcnt(0)" ::: "memory");
#pragma unroll
    for (int e = 0; e < 4; ++e) {
      const int item = (it * 16 + pb) * 512 + (tid & ~63) + 16 * e + (l >> 2), P = item / 160, nq = item % 160;
      const unsigned o = P < 188 ? (nq / 20) * WT_TILE + P * 1280 + (nq % 20) * 64 + (l & 3) * 16 : 0xF0000000u;
      st128(rw, o, *(const LAS u32x4*)(tb + e * 1024 + l * 16));
    }
    asm volatile("s_waitcnt lgkmcnt(0)" ::: "memory");
  }
}

constexpr int M_TR = 65536, M_FR = 131072, M_KV = 0, KV_SLOT = 3776, KV_ONES = 3264, M_LN = M_KV + 6 * KV_SLOT, M_GO = 36864, M_ZERO = M_GO + 16, M_Q = 24576, M_BYTES = 163840;
constexpr int O_W = 57344;

DI constexpr int z_s(int st) { return st == 9 ? 4 : st % 6; }
DI constexpr int z_t(int st) { return z_s(st) == 3 ? 0 : z_s(st) == 5 ? 40960 : M_TR + (z_s(st) == 4 ? 0 : z_s(st) + 1) * 16384; }
DI constexpr int z_f(int st) { return z_s(st) == 3 ? 16384 : z_s(st) == 5 ? 57344 : M_FR + (z_s(st) == 4 ? 0 : z_s(st) + 1) * 8192; }
#define PIN(x) asm volatile("" : "+v"(x))
#define MIX_B1 \
  "s_mov_b32 %[cnt], 8\n" \
  "1:\n" \
  "s_waitcnt lgkmcnt(1)\n" \
  "v_mfma_f32_32x32x16_bf16 v[32:47], v[88:91], v[80:83], 0\n" \
  "s_barrier\n" \
  "v_exp_f32 v16, v16\n" \
  "v_exp_f32 v17, v17\n" \
  "v_exp_f32 v18, v18\n" \
  "ds_read_b128 v[112:115], v128 offset:32768\n" \
  "ds_read_b128 v[108:111], v127 offset:16384\n" \
  "v_mfma_f32_32x32x16_bf16 v[48:63], v[96:99], v[104:107], v[48:63]\n" \
  "v_cvt_pk_bf16_f32 v100, v16, v17\n" \
  "v_exp_f32 v19, v19\n" \
  "v_exp_f32 v20, v20\n" \
  "v_cvt_pk_bf16_f32 v101, v18, v19\n" \
  "ds_read_b128 v[120:123], v129 offset:32768\n" \
  "ds_read_b128 v[116:119], v127 offset:18432\n" \
  "s_waitcnt lgkmcnt(4)\n" \
  "v_mfma_f32_32x32x8_bf16 v[32:47], v[86:87], v[84:85], v[32:47]\n" \
  "v_exp_f32 v21, v21\n" \
  "v_exp_f32 v22, v22\n" \
  "v_exp_f32 v23, v23\n" \
  "ds_read_b64_tr_b16 v[92:93], v126 offset:18880\n" \
  "ds_read_b64_tr_b16 v[94:95], v126 offset:19008\n" \
  "s_waitcnt lgkmcnt(4)\n" \
  "v_mfma_f32_32x32x16_bf16 v[64:79], v[108:111], v[112:115], v[64:79]\n" \
  "v_cvt_pk_bf16_f32 v102, v20, v21\n" \
  "v_cvt_pk_bf16_f32 v103, v22, v23\n" \
  "v_exp_f32 v24, v24\n" \
  "v_exp_f32 v25, v25\n" \
  "ds_read_b128 v[112:115], v130 offset:32768\n" \
  "ds_read_b128 v[108:111], v127 offset:20480\n" \
  "s_waitcnt lgkmcnt(2)\n" \
  "v_mfma_f32_32x32x16_bf16 v[48:63], v[92:95], v[100:103], v[48:63]\n" \
  "v_exp_f32 v26, v26\n" \
  "v_exp_f32 v27, v27\n" \
  "v_cvt_pk_bf16_f32 v104, v24, v25\n" \
  "v_cvt_pk_bf16_f32 v105, v26, v27\n" \
  "ds_read_b64_tr_b16 v[96:97], v126 offset:19136\n" \
  "ds_read_b64_tr_b16 v[98:99], v126 offset:19264\n" \
  "v_mfma_f32_32x32x16_bf16 v[64:79], v[116:119], v[120:123], v[64:79]\n" \
  "v_exp_f32 v28, v28\n" \
  "v_exp_f32 v29, v29\n" \
  "v_exp_f32 v30, v30\n" \
  "ds_read_b128 v[88:91], v124 offset:0\n" \
  "ds_read_b64 v[86:87], v125 offset:0\n" \
  "s_waitcnt lgkmcnt(4)\n" \
  "v_mfma_f32_32x32x16_bf16 v[64:79], v[108:111], v[112:115], v[64:79]\n" \
  "v_exp_f32 v31, v31\n" \
  "v_cvt_pk_bf16_f32 v106, v28, v29\n" \
  "v_cvt_pk_bf16_f32 v107, v30, v31\n" \
  "v_exp_f32 v32, v32\n" \
  "ds_read_b128 v[120:123], v131 offset:32768\n" \
  "ds_read_b128 v[116:119], v127 offset:22528\n" \
  "s_waitcnt lgkmcnt(3)\n" \
  "v_mfma_f32_32x32x16_bf16 v[16:31], v[88:91], v[80:83], 0\n" \
  "v_exp_f32 v33, v33\n" \
  "v_exp_f32 v34, v34\n" \
  "v_exp_f32 v35, v35\n" \
  "ds_read_b64_tr_b16 v[92:93], v126 offset:0\n" \
  "ds_read_b64_tr_b16 v[94:95], v126 offset:128\n" \
  "v_mfma_f32_32x32x16_bf16 v[48:63], v[96:99], v[104:107], v[48:63]\n" \
  "v_cvt_pk_bf16_f32 v100, v32, v33\n" \
  "v_cvt_pk_bf16_f32 v101, v34, v35\n" \
  "v_exp_f32 v36, v36\n" \
  "v_exp_f32 v37, v37\n" \
  "s_waitcnt lgkmcnt(2)\n" \
  "v_mfma_f32_32x32x16_bf16 v[64:79], v[116:119], v[120:123], v[64:79]\n" \
  "v_exp_f32 v38, v38\n" \
  "v_exp_f32 v39, v39\n" \
  "v_cvt_pk_bf16_f32 v102, v36, v37\n" \
  "v_cvt_pk_bf16_f32 v103, v38, v39\n" \
  "v_mfma_f32_32x32x8_bf16 v[16:31], v[86:87], v[84:85], v[16:31]\n" \
  "s_barrier\n" \
  "v_exp_f32 v40, v40\n" \
  "v_exp_f32 v41, v41\n" \
  "v_exp_f32 v42, v42\n" \
  "ds_read_b128 v[112:115], v128 offset:49152\n" \
  "ds_read_b128 v[108:111], v127 offset:24576\n" \
  "s_waitcnt lgkmcnt(2)\n" \
  "v_mfma_f32_32x32x16_bf16 v[48:63], v[92:95], v[100:103], v[48:63]\n" \
  "v_cvt_pk_bf16_f32 v104, v40, v41\n" \
  "v_exp_f32 v43, v43\n" \
  "v_exp_f32 v44, v44\n" \
  "v_cvt_pk_bf16_f32 v105, v42, v43\n" \
  "ds_read_b128 v[120:123], v129 offset:49152\n" \
  "ds_read_b128 v[116:119], v127 offset:26624\n" \
  "s_waitcnt lgkmcnt(2)\n" \
  "v_mfma_f32_32x32x16_bf16 v[64:79], v[108:111], v[112:115], v[64:79]\n" \
  "v_exp_f32 v45, v45\n" \
  "v_exp_f32 v46, v46\n" \
  "v_exp_f32 v47, v47\n" \
  "ds_read_b128 v[88:91], v124 offset:3776\n" \
  "ds_read_b64 v[86:87], v125 offset:3776\n" \
  "s_waitcnt lgkmcnt(2)\n" \
  "v_mfma_f32_32x32x16_bf16 v[64:79], v[116:119], v[120:123], v[64:79]\n" \
  "v_cvt_pk_bf16_f32 v106, v44, v45\n" \
  "v_cvt_pk_bf16_f32 v107, v46, v47\n" \
  "v_exp_f32 v16, v16\n" \
  "v_exp_f32 v17, v17\n" \
  "ds_read_b64_tr_b16 v[96:97], v126 offset:256\n" \
  "ds_read_b64_tr_b16 v[98:99], v126 offset:384\n" \
  "s_waitcnt lgkmcnt(3)\n" \
  "v_mfma_f32_32x32x16_bf16 v[32:47], v[88:91], v[80:83], 0\n" \
  "v_exp_f32 v18, v18\n" \
  "v_exp_f32 v19, v19\n" \
  "v_cvt_pk_bf16_f32 v100, v16, v17\n" \
  "v_cvt_pk_bf16_f32 v101, v18, v19\n" \
  "ds_read_b128 v[112:115], v130 offset:49152\n" \
  "ds_read_b128 v[108:111], v127 offset:28672\n" \
  "s_waitcnt lgkmcnt(2)\n" \
  "v_mfma_f32_32x32x16_bf16 v[48:63], v[96:99], v[104:107], v[48:63]\n" \
  "v_exp_f32 v20, v20\n" \
  "v_exp_f32 v21, v21\n" \
  "v_exp_f32 v22, v22\n" \
  "ds_read_b64_tr_b16 v[92:93], v126 offset:3776\n" \
  "ds_read_b64_tr_b16 v[94:95], v126 offset:3904\n" \
  "v_mfma_f32_32x32x8_bf16 v[32:47], v[86:87], v[84:85], v[32:47]\n" \
  "v_cvt_pk_bf16_f32 v102, v20, v21\n" \
  "v_exp_f32 v23, v23\n" \
  "v_exp_f32 v24, v24\n" \
  "v_cvt_pk_bf16_f32 v103, v22, v23\n" \
  "ds_read_b128 v[120:123], v131 offset:49152\n" \
  "ds_read_b128 v[116:119], v127 offset:30720\n" \
  "s_waitcnt lgkmcnt(4)\n" \
  "v_mfma_f32_32x32x16_bf16 v[64:79], v[108:111], v[112:115], v[64:79]\n" \
  "v_exp_f32 v25, v25\n" \
  "v_exp_f32 v26, v26\n" \
  "v_exp_f32 v27, v27\n" \
  "ds_read_b64_tr_b16 v[96:97], v126 offset:4032\n" \
  "ds_read_b64_tr_b16 v[98:99], v126 offset:4160\n" \
  "s_waitcnt lgkmcnt(4)\n" \
  "v_mfma_f32_32x32x16_bf16 v[48:63], v[92:95], v[100:103], v[48:63]\n" \
  "v_cvt_pk_bf16_f32 v104, v24, v25\n" \
  "v_cvt_pk_bf16_f32 v105, v26, v27\n" \
  "v_exp_f32 v28, v28\n" \
  "v_exp_f32 v29, v29\n" \
  "ds_read_b128 v[88:91], v124 offset:7552\n" \
  "ds_read_b64 v[86:87], v125 offset:7552\n" \
  "s_waitcnt lgkmcnt(4)\n" \
  "v_mfma_f32_32x32x16_bf16 v[64:79], v[116:119], v[120:123], v[64:79]\n" \
  "v_exp_f32 v30, v30\n" \
  "v_exp_f32 v31, v31\n" \
  "v_cvt_pk_bf16_f32 v106, v28, v29\n" \
  "v_cvt_pk_bf16_f32 v107, v30, v31\n" \
  "s_waitcnt lgkmcnt(1)\n" \
  "v_mfma_f32_32x32x16_bf16 v[16:31], v[88:91], v[80:83], 0\n" \
  "s_barrier\n" \
  "v_exp_f32 v32, v32\n" \
  "v_exp_f32 v33, v33\n" \
  "v_exp_f32 v34, v34\n" \
  "ds_read_b128 v[112:115], v128 offset:0\n" \
  "ds_read_b128 v[108:111], v127 offset:0\n" \
  "v_mfma_f32_32x32x16_bf16 v[48:63], v[96:99], v[104:107], v[48:63]\n" \
  "v_cvt_pk_bf16_f32 v100, v32, v33\n" \
  "v_exp_f32 v35, v35\n" \
  "v_exp_f32 v36, v36\n" \
  "v_cvt_pk_bf16_f32 v101, v34, v35\n" \
  "ds_read_b128 v[120:123], v129 offset:0\n" \
  "ds_read_b128 v[116:119], v127 offset:2048\n" \
  "s_waitcnt lgkmcnt(4)\n" \
  "v_mfma_f32_32x32x8_bf16 v[16:31], v[86:87], v[84:85], v[16:31]\n" \
  "v_exp_f32 v37, v37\n" \
  "v_exp_f32 v38, v38\n" \
  "v_exp_f32 v39, v39\n" \
  "ds_read_b64_tr_b16 v[92:93], v126 offset:7552\n" \
  "ds_read_b64_tr_b16 v[94:95], v126 offset:7680\n" \
  "s_waitcnt lgkmcnt(4)\n" \
  "v_mfma_f32_32x32x16_bf16 v[64:79], v[108:111], v[112:115], v[64:79]\n" \
  "v_cvt_pk_bf16_f32 v102, v36, v37\n" \
  "v_cvt_pk_bf16_f32 v103, v38, v39\n" \
  "v_exp_f32 v40, v40\n" \
  "v_exp_f32 v41, v41\n" \
  "ds_read_b128 v[112:115], v130 offset:0\n" \
  "ds_read_b128 v[108:111], v127 offset:4096\n" \
  "s_waitcnt lgkmcnt(2)\n" \
  "v_mfma_f32_32x32x16_bf16 v[48:63], v[92:95], v[100:103], v[48:63]\n" \
  "v_exp_f32 v42, v42\n" \
  "v_exp_f32 v43, v43\n" \
  "v_cvt_pk_bf16_f32 v104, v40, v41\n" \
  "v_cvt_pk_bf16_f32 v105, v42, v43\n" \
  "ds_read_b64_tr_b16 v[96:97], v126 offset:7808\n" \
  "ds_read_b64_tr_b16 v[98:99], v126 offset:7936\n" \
  "v_mfma_f32_32x32x16_bf16 v[64:79], v[116:119], v[120:123], v[64:79]\n" \
  "v_exp_f32 v44, v44\n" \
  "v_exp_f32 v45, v45\n" \
  "v_exp_f32 v46, v46\n" \
  "ds_read_b128 v[88:91], v124 offset:11328\n" \
  "ds_read_b64 v[86:87], v125 offset:11328\n" \
  "s_waitcnt lgkmcnt(4)\n" \
  "v_mfma_f32_32x32x16_bf16 v[64:79], v[108:111], v[112:115], v[64:79]\n" \
  "v_exp_f32 v47, v47\n" \
  "v_cvt_pk_bf16_f32 v106, v44, v45\n" \
  "v_cvt_pk_bf16_f32 v107, v46, v47\n" \
  "v_exp_f32 v16, v16\n" \
  "ds_read_b128 v[120:123], v131 offset:0\n" \
  "ds_read_b128 v[116:119], v127 offset:6144\n" \
  "s_waitcnt lgkmcnt(3)\n" \
  "v_mfma_f32_32x32x16_bf16 v[32:47], v[88:91], v[80:83], 0\n" \
  "v_exp_f32 v17, v17\n" \
  "v_exp_f32 v18, v18\n" \
  "v_exp_f32 v19, v19\n" \
  "ds_read_b64_tr_b16 v[92:93], v126 offset:11328\n" \
  "ds_read_b64_tr_b16 v[94:95], v126 offset:11456\n" \
  "v_mfma_f32_32x32x16_bf16 v[48:63], v[96:99], v[104:107], v[48:63]\n" \
  "v_cvt_pk_bf16_f32 v100, v16, v17\n" \
  "v_cvt_pk_bf16_f32 v101, v18, v19\n" \
  "v_exp_f32 v20, v20\n" \
  "v_exp_f32 v21, v21\n" \
  "s_waitcnt lgkmcnt(2)\n" \
  "v_mfma_f32_32x32x16_bf16 v[64:79], v[116:119], v[120:123], v[64:79]\n" \
  "v_exp_f32 v22, v22\n" \
  "v_exp_f32 v23, v23\n" \
  "v_cvt_pk_bf16_f32 v102, v20, v21\n" \
  "v_cvt_pk_bf16_f32 v103, v22, v23\n" \
  "v_mfma_f32_32x32x8_bf16 v[32:47], v[86:87], v[84:85], v[32:47]\n" \
  "s_barrier\n" \
  "v_exp_f32 v24, v24\n" \
  "v_exp_f32 v25, v25\n" \
  "v_exp_f32 v26, v26\n" \
  "ds_read_b128 v[112:115], v128 offset:16384\n" \
  "ds_read_b128 v[108:111], v127 offset:8192\n" \
  "s_waitcnt lgkmcnt(2)\n" \
  "v_mfma_f32_32x32x16_bf16 v[48:63], v[92:95], v[100:103], v[48:63]\n" \
  "v_cvt_pk_bf16_f32 v104, v24, v25\n" \
  "v_exp_f32 v27, v27\n" \
  "v_exp_f32 v28, v28\n" \
  "v_cvt_pk_bf16_f32 v105, v26, v27\n" \
  "ds_read_b128 v[120:123], v129 offset:16384\n" \
  "ds_read_b128 v[116:119], v127 offset:10240\n" \
  "s_waitcnt lgkmcnt(2)\n" \
  "v_mfma_f32_32x32x16_bf16 v[64:79], v[108:111], v[112:115], v[64:79]\n" \
  "v_exp_f32 v29, v29\n" \
  "v_exp_f32 v30, v30\n" \
  "v_exp_f32 v31, v31\n" \
  "ds_read_b128 v[88:91], v124 offset:15104\n" \
  "ds_read_b64 v[86:87], v125 offset:15104\n" \
  "s_waitcnt lgkmcnt(2)\n" \
  "v_mfma_f32_32x32x16_bf16 v[64:79], v[116:119], v[120:123], v[64:79]\n" \
  "v_cvt_pk_bf16_f32 v106, v28, v29\n" \
  "v_cvt_pk_bf16_f32 v107, v30, v31\n" \
  "v_exp_f32 v32, v32\n" \
  "v_exp_f32 v33, v33\n" \
  "ds_read_b64_tr_b16 v[96:97], v126 offset:11584\n" \
  "ds_read_b64_tr_b16 v[98:99], v126 offset:11712\n" \
  "s_waitcnt lgkmcnt(3)\n" \
  "v_mfma_f32_32x32x16_bf16 v[16:31], v[88:91], v[80:83], 0\n" \
  "v_exp_f32 v34, v34\n" \
  "v_exp_f32 v35, v35\n" \
  "v_cvt_pk_bf16_f32 v100, v32, v33\n" \
  "v_cvt_pk_bf16_f32 v101, v34, v35\n" \
  "ds_read_b128 v[112:115], v130 offset:16384\n" \
  "ds_read_b128 v[108:111], v127 offset:12288\n" \
  "s_waitcnt lgkmcnt(2)\n" \
  "v_mfma_f32_32x32x16_bf16 v[48:63], v[96:99], v[104:107], v[48:63]\n" \
  "v_exp_f32 v36, v36\n" \
  "v_exp_f32 v37, v37\n" \
  "v_exp_f32 v38, v38\n" \
  "ds_read_b64_tr_b16 v[92:93], v126 offset:15104\n" \
  "ds_read_b64_tr_b16 v[94:95], v126 offset:15232\n" \
  "v_mfma_f32_32x32x8_bf16 v[16:31], v[86:87], v[84:85], v[16:31]\n" \
  "v_cvt_pk_bf16_f32 v102, v36, v37\n" \
  "v_exp_f32 v39, v39\n" \
  "v_exp_f32 v40, v40\n" \
  "v_cvt_pk_bf16_f32 v103, v38, v39\n" \
  "ds_read_b128 v[120:123], v131 offset:16384\n" \
  "ds_read_b128 v[116:119], v127 offset:14336\n" \
  "s_waitcnt lgkmcnt(4)\n" \
  "v_mfma_f32_32x32x16_bf16 v[64:79], v[108:111], v[112:115], v[64:79]\n" \
  "v_exp_f32 v41, v41\n" \
  "v_exp_f32 v42, v42\n" \
  "v_exp_f32 v43, v43\n" \
  "ds_read_b64_tr_b16 v[96:97], v126 offset:15360\n" \
  "ds_read_b64_tr_b16 v[98:99], v126 offset:15488\n" \
  "s_waitcnt lgkmcnt(4)\n" \
  "v_mfma_f32_32x32x16_bf16 v[48:63], v[92:95], v[100:103], v[48:63]\n" \
  "v_cvt_pk_bf16_f32 v104, v40, v41\n" \
  "v_cvt_pk_bf16_f32 v105, v42, v43\n" \
  "v_exp_f32 v44, v44\n" \
  "v_exp_f32 v45, v45\n" \
  "ds_read_b128 v[88:91], v124 offset:18880\n" \
  "ds_read_b64 v[86:87], v125 offset:18880\n" \
  "s_waitcnt lgkmcnt(4)\n" \
  "v_mfma_f32_32x32x16_bf16 v[64:79], v[116:119], v[120:123], v[64:79]\n" \
  "v_exp_f32 v46, v46\n" \
  "v_exp_f32 v47, v47\n" \
  "v_cvt_pk_bf16_f32 v106, v44, v45\n" \
  "v_cvt_pk_bf16_f32 v107, v46, v47\n" \
  "s_sub_u32 %[cnt], %[cnt], 1\n" \
  "s_cmp_lg_u32 %[cnt], 0\n" \
  "s_cbranch_scc1 1b\n" \
  "s_waitcnt lgkmcnt(2)\n" \
  "s_nop 0\n" \
  "v_mfma_f32_32x32x16_bf16 v[48:63], v[96:99], v[104:107], v[48:63]\n" \
  "s_waitcnt lgkmcnt(0)\n" \
  "s_barrier\n" \
  "s_nop 15\n" \
  ""
#define MIX_A0 \
  "ds_read_b128 v[80:83], v100\n" \
  "ds_read_b64 v[84:85], v101\n" \
  "ds_read_b128 v[88:91], v124 offset:0\n" \
  "ds_read_b64 v[86:87], v125 offset:0\n" \
  "ds_read_b128 v[108:111], v124 offset:3776\n" \
  "ds_read_b64 v[116:117], v125 offset:3776\n" \
  "ds_read_b64_tr_b16 v[92:93], v126 offset:3776\n" \
  "ds_read_b64_tr_b16 v[94:95], v126 offset:3904\n" \
  "ds_read_b64_tr_b16 v[96:97], v126 offset:4032\n" \
  "ds_read_b64_tr_b16 v[98:99], v126 offset:4160\n" \
  "s_waitcnt lgkmcnt(7)\n" \
  "v_mfma_f32_32x32x16_bf16 v[16:31], v[88:91], v[80:83], 0\n" \
  "s_waitcnt lgkmcnt(6)\n" \
  "v_mfma_f32_32x32x8_bf16 v[16:31], v[86:87], v[84:85], v[16:31]\n" \
  "s_waitcnt lgkmcnt(5)\n" \
  "v_mfma_f32_32x32x16_bf16 v[32:47], v[108:111], v[80:83], 0\n" \
  "s_waitcnt lgkmcnt(4)\n" \
  "v_mfma_f32_32x32x8_bf16 v[32:47], v[116:117], v[84:85], v[32:47]\n" \
  "ds_read_b128 v[88:91], v124 offset:7552\n" \
  "ds_read_b64 v[86:87], v125 offset:7552\n" \
  "s_nop 7\n" \
  "v_exp_f32 v16, v16\n" \
  "v_exp_f32 v17, v17\n" \
  "v_exp_f32 v18, v18\n" \
  "v_exp_f32 v19, v19\n" \
  "v_cvt_pk_bf16_f32 v100, v16, v17\n" \
  "v_exp_f32 v20, v20\n" \
  "v_exp_f32 v21, v21\n" \
  "v_cvt_pk_bf16_f32 v101, v18, v19\n" \
  "v_exp_f32 v22, v22\n" \
  "v_exp_f32 v23, v23\n" \
  "v_cvt_pk_bf16_f32 v102, v20, v21\n" \
  "v_exp_f32 v24, v24\n" \
  "v_exp_f32 v25, v25\n" \
  "v_cvt_pk_bf16_f32 v103, v22, v23\n" \
  "v_exp_f32 v26, v26\n" \
  "v_exp_f32 v27, v27\n" \
  "v_cvt_pk_bf16_f32 v104, v24, v25\n" \
  "s_waitcnt lgkmcnt(4)\n" \
  "v_mfma_f32_32x32x16_bf16 v[48:63], v[92:95], v[100:103], 0\n" \
  "v_exp_f32 v28, v28\n" \
  "v_exp_f32 v29, v29\n" \
  "v_cvt_pk_bf16_f32 v105, v26, v27\n" \
  "v_exp_f32 v30, v30\n" \
  "v_exp_f32 v31, v31\n" \
  "v_cvt_pk_bf16_f32 v106, v28, v29\n" \
  "s_nop 0\n" \
  "v_cvt_pk_bf16_f32 v107, v30, v31\n" \
  "s_waitcnt lgkmcnt(0)\n" \
  "s_barrier\n" \
  "s_mov_b32 %[cnt], 2\n" \
  "1:\n" \
  "s_waitcnt lgkmcnt(1)\n" \
  "v_mfma_f32_32x32x16_bf16 v[16:31], v[88:91], v[80:83], 0\n" \
  "ds_read_b64_tr_b16 v[92:93], v126 offset:7552\n" \
  "ds_read_b64_tr_b16 v[94:95], v126 offset:7680\n" \
  "v_exp_f32 v32, v32\n" \
  "v_exp_f32 v33, v33\n" \
  "v_exp_f32 v34, v34\n" \
  "v_exp_f32 v35, v35\n" \
  "v_cvt_pk_bf16_f32 v100, v32, v33\n" \
  "v_cvt_pk_bf16_f32 v101, v34, v35\n" \
  "v_mfma_f32_32x32x16_bf16 v[48:63], v[96:99], v[104:107], v[48:63]\n" \
  "ds_read_b64_tr_b16 v[96:97], v126 offset:7808\n" \
  "ds_read_b64_tr_b16 v[98:99], v126 offset:7936\n" \
  "v_exp_f32 v36, v36\n" \
  "v_exp_f32 v37, v37\n" \
  "v_exp_f32 v38, v38\n" \
  "v_exp_f32 v39, v39\n" \
  "v_cvt_pk_bf16_f32 v102, v36, v37\n" \
  "v_cvt_pk_bf16_f32 v103, v38, v39\n" \
  "s_waitcnt lgkmcnt(4)\n" \
  "v_mfma_f32_32x32x8_bf16 v[16:31], v[86:87], v[84:85], v[16:31]\n" \
  "ds_read_b128 v[88:91], v124 offset:11328\n" \
  "ds_read_b64 v[86:87], v125 offset:11328\n" \
  "v_exp_f32 v40, v40\n" \
  "v_exp_f32 v41, v41\n" \
  "v_exp_f32 v42, v42\n" \
  "v_exp_f32 v43, v43\n" \
  "v_cvt_pk_bf16_f32 v104, v40, v41\n" \
  "v_cvt_pk_bf16_f32 v105, v42, v43\n" \
  "s_waitcnt lgkmcnt(4)\n" \
  "v_mfma_f32_32x32x16_bf16 v[48:63], v[92:95], v[100:103], v[48:63]\n" \
  "v_exp_f32 v44, v44\n" \
  "v_exp_f32 v45, v45\n" \
  "v_exp_f32 v46, v46\n" \
  "v_exp_f32 v47, v47\n" \
  "v_cvt_pk_bf16_f32 v106, v44, v45\n" \
  "v_cvt_pk_bf16_f32 v107, v46, v47\n" \
  "s_waitcnt lgkmcnt(1)\n" \
  "v_mfma_f32_32x32x16_bf16 v[32:47], v[88:91], v[80:83], 0\n" \
  "ds_read_b64_tr_b16 v[92:93], v126 offset:11328\n" \
  "ds_read_b64_tr_b16 v[94:95], v126 offset:11456\n" \
  "v_exp_f32 v16, v16\n" \
  "v_exp_f32 v17, v17\n" \
  "v_exp_f32 v18, v18\n" \
  "v_exp_f32 v19, v19\n" \
  "v_cvt_pk_bf16_f32 v100, v16, v17\n" \
  "v_cvt_pk_bf16_f32 v101, v18, v19\n" \
  "v_mfma_f32_32x32x16_bf16 v[48:63], v[96:99], v[104:107], v[48:63]\n" \
  "ds_read_b64_tr_b16 v[96:97], v126 offset:11584\n" \
  "ds_read_b64_tr_b16 v[98:99], v126 offset:11712\n" \
  "v_exp_f32 v20, v20\n" \
  "v_exp_f32 v21, v21\n" \
  "v_exp_f32 v22, v22\n" \
  "v_exp_f32 v23, v23\n" \
  "v_cvt_pk_bf16_f32 v102, v20, v21\n" \
  "v_cvt_pk_bf16_f32 v103, v22, v23\n" \
  "s_barrier\n" \
  "s_waitcnt lgkmcnt(4)\n" \
  "v_mfma_f32_32x32x8_bf16 v[32:47], v[86:87], v[84:85], v[32:47]\n" \
  "ds_read_b128 v[88:91], v124 offset:15104\n" \
  "ds_read_b64 v[86:87], v125 offset:15104\n" \
  "v_exp_f32 v24, v24\n" \
  "v_exp_f32 v25, v25\n" \
  "v_exp_f32 v26, v26\n" \
  "v_exp_f32 v27, v27\n" \
  "v_cvt_pk_bf16_f32 v104, v24, v25\n" \
  "v_cvt_pk_bf16_f32 v105, v26, v27\n" \
  "s_waitcnt lgkmcnt(4)\n" \
  "v_mfma_f32_32x32x16_bf16 v[48:63], v[92:95], v[100:103], v[48:63]\n" \
  "v_exp_f32 v28, v28\n" \
  "v_exp_f32 v29, v29\n" \
  "v_exp_f32 v30, v30\n" \
  "v_exp_f32 v31, v31\n" \
  "v_cvt_pk_bf16_f32 v106, v28, v29\n" \
  "v_cvt_pk_bf16_f32 v107, v30, v31\n" \
  "s_waitcnt lgkmcnt(1)\n" \
  "v_mfma_f32_32x32x16_bf16 v[16:31], v[88:91], v[80:83], 0\n" \
  "ds_read_b64_tr_b16 v[92:93], v126 offset:15104\n" \
  "ds_read_b64_tr_b16 v[94:95], v126 offset:15232\n" \
  "v_exp_f32 v32, v32\n" \
  "v_exp_f32 v33, v33\n" \
  "v_exp_f32 v34, v34\n" \
  "v_exp_f32 v35, v35\n" \
  "v_cvt_pk_bf16_f32 v100, v32, v33\n" \
  "v_cvt_pk_bf16_f32 v101, v34, v35\n" \
  "v_mfma_f32_32x32x16_bf16 v[48:63], v[96:99], v[104:107], v[48:63]\n" \
  "ds_read_b64_tr_b16 v[96:97], v126 offset:15360\n" \
  "ds_read_b64_tr_b16 v[98:99], v126 offset:15488\n" \
  "v_exp_f32 v36, v36\n" \
  "v_exp_f32 v37, v37\n" \
  "v_exp_f32 v38, v38\n" \
  "v_exp_f32 v39, v39\n" \
  "v_cvt_pk_bf16_f32 v102, v36, v37\n" \
  "v_cvt_pk_bf16_f32 v103, v38, v39\n" \
  "s_waitcnt lgkmcnt(4)\n" \
  "v_mfma_f32_32x32x8_bf16 v[16:31], v[86:87], v[84:85], v[16:31]\n" \
  "ds_read_b128 v[88:91], v124 offset:18880\n" \
  "ds_read_b64 v[86:87], v125 offset:18880\n" \
  "v_exp_f32 v40, v40\n" \
  "v_exp_f32 v41, v41\n" \
  "v_exp_f32 v42, v42\n" \
  "v_exp_f32 v43, v43\n" \
  "v_cvt_pk_bf16_f32 v104, v40, v41\n" \
  "v_cvt_pk_bf16_f32 v105, v42, v43\n" \
  "s_waitcnt lgkmcnt(4)\n" \
  "v_mfma_f32_32x32x16_bf16 v[48:63], v[92:95], v[100:103], v[48:63]\n" \
  "v_exp_f32 v44, v44\n" \
  "v_exp_f32 v45, v45\n" \
  "v_exp_f32 v46, v46\n" \
  "v_exp_f32 v47, v47\n" \
  "v_cvt_pk_bf16_f32 v106, v44, v45\n" \
  "v_cvt_pk_bf16_f32 v107, v46, v47\n" \
  "s_waitcnt lgkmcnt(2)\n" \
  "s_barrier\n" \
  "s_waitcnt lgkmcnt(1)\n" \
  "v_mfma_f32_32x32x16_bf16 v[32:47], v[88:91], v[80:83], 0\n" \
  "ds_read_b64_tr_b16 v[92:93], v126 offset:18880\n" \
  "ds_read_b64_tr_b16 v[94:95], v126 offset:19008\n" \
  "v_exp_f32 v16, v16\n" \
  "v_exp_f32 v17, v17\n" \
  "v_exp_f32 v18, v18\n" \
  "v_exp_f32 v19, v19\n" \
  "v_cvt_pk_bf16_f32 v100, v16, v17\n" \
  "v_cvt_pk_bf16_f32 v101, v18, v19\n" \
  "v_mfma_f32_32x32x16_bf16 v[48:63], v[96:99], v[104:107], v[48:63]\n" \
  "ds_read_b64_tr_b16 v[96:97], v126 offset:19136\n" \
  "ds_read_b64_tr_b16 v[98:99], v126 offset:19264\n" \
  "v_exp_f32 v20, v20\n" \
  "v_exp_f32 v21, v21\n" \
  "v_exp_f32 v22, v22\n" \
  "v_exp_f32 v23, v23\n" \
  "v_cvt_pk_bf16_f32 v102, v20, v21\n" \
  "v_cvt_pk_bf16_f32 v103, v22, v23\n" \
  "s_waitcnt lgkmcnt(4)\n" \
  "v_mfma_f32_32x32x8_bf16 v[32:47], v[86:87], v[84:85], v[32:47]\n" \
  "ds_read_b128 v[88:91], v124 offset:0\n" \
  "ds_read_b64 v[86:87], v125 offset:0\n" \
  "v_exp_f32 v24, v24\n" \
  "v_exp_f32 v25, v25\n" \
  "v_exp_f32 v26, v26\n" \
  "v_exp_f32 v27, v27\n" \
  "v_cvt_pk_bf16_f32 v104, v24, v25\n" \
  "v_cvt_pk_bf16_f32 v105, v26, v27\n" \
  "s_waitcnt lgkmcnt(4)\n" \
  "v_mfma_f32_32x32x16_bf16 v[48:63], v[92:95], v[100:103], v[48:63]\n" \
  "v_exp_f32 v28, v28\n" \
  "v_exp_f32 v29, v29\n" \
  "v_exp_f32 v30, v30\n" \
  "v_exp_f32 v31, v31\n" \
  "v_cvt_pk_bf16_f32 v106, v28, v29\n" \
  "v_cvt_pk_bf16_f32 v107, v30, v31\n" \
  "s_waitcnt lgkmcnt(1)\n" \
  "v_mfma_f32_32x32x16_bf16 v[16:31], v[88:91], v[80:83], 0\n" \
  "ds_read_b64_tr_b16 v[92:93], v126 offset:0\n" \
  "ds_read_b64_tr_b16 v[94:95], v126 offset:128\n" \
  "v_exp_f32 v32, v32\n" \
  "v_exp_f32 v33, v33\n" \
  "v_exp_f32 v34, v34\n" \
  "v_exp_f32 v35, v35\n" \
  "v_cvt_pk_bf16_f32 v100, v32, v33\n" \
  "v_cvt_pk_bf16_f32 v101, v34, v35\n" \
  "v_mfma_f32_32x32x16_bf16 v[48:63], v[96:99], v[104:107], v[48:63]\n" \
  "ds_read_b64_tr_b16 v[96:97], v126 offset:256\n" \
  "ds_read_b64_tr_b16 v[98:99], v126 offset:384\n" \
  "v_exp_f32 v36, v36\n" \
  "v_exp_f32 v37, v37\n" \
  "v_exp_f32 v38, v38\n" \
  "v_exp_f32 v39, v39\n" \
  "v_cvt_pk_bf16_f32 v102, v36, v37\n" \
  "v_cvt_pk_bf16_f32 v103, v38, v39\n" \
  "s_barrier\n" \
  "s_waitcnt lgkmcnt(4)\n" \
  "v_mfma_f32_32x32x8_bf16 v[16:31], v[86:87], v[84:85], v[16:31]\n" \
  "ds_read_b128 v[88:91], v124 offset:3776\n" \
  "ds_read_b64 v[86:87], v125 offset:3776\n" \
  "v_exp_f32 v40, v40\n" \
  "v_exp_f32 v41, v41\n" \
  "v_exp_f32 v42, v42\n" \
  "v_exp_f32 v43, v43\n" \
  "v_cvt_pk_bf16_f32 v104, v40, v41\n" \
  "v_cvt_pk_bf16_f32 v105, v42, v43\n" \
  "s_waitcnt lgkmcnt(4)\n" \
  "v_mfma_f32_32x32x16_bf16 v[48:63], v[92:95], v[100:103], v[48:63]\n" \
  "v_exp_f32 v44, v44\n" \
  "v_exp_f32 v45, v45\n" \
  "v_exp_f32 v46, v46\n" \
  "v_exp_f32 v47, v47\n" \
  "v_cvt_pk_bf16_f32 v106, v44, v45\n" \
  "v_cvt_pk_bf16_f32 v107, v46, v47\n" \
  "s_waitcnt lgkmcnt(1)\n" \
  "v_mfma_f32_32x32x16_bf16 v[32:47], v[88:91], v[80:83], 0\n" \
  "ds_read_b64_tr_b16 v[92:93], v126 offset:3776\n" \
  "ds_read_b64_tr_b16 v[94:95], v126 offset:3904\n" \
  "v_exp_f32 v16, v16\n" \
  "v_exp_f32 v17, v17\n" \
  "v_exp_f32 v18, v18\n" \
  "v_exp_f32 v19, v19\n" \
  "v_cvt_pk_bf16_f32 v100, v16, v17\n" \
  "v_cvt_pk_bf16_f32 v101, v18, v19\n" \
  "v_mfma_f32_32x32x16_bf16 v[48:63], v[96:99], v[104:107], v[48:63]\n" \
  "ds_read_b64_tr_b16 v[96:97], v126 offset:4032\n" \
  "ds_read_b64_tr_b16 v[98:99], v126 offset:4160\n" \
  "v_exp_f32 v20, v20\n" \
  "v_exp_f32 v21, v21\n" \
  "v_exp_f32 v22, v22\n" \
  "v_exp_f32 v23, v23\n" \
  "v_cvt_pk_bf16_f32 v102, v20, v21\n" \
  "v_cvt_pk_bf16_f32 v103, v22, v23\n" \
  "s_waitcnt lgkmcnt(4)\n" \
  "v_mfma_f32_32x32x8_bf16 v[32:47], v[86:87], v[84:85], v[32:47]\n" \
  "ds_read_b128 v[88:91], v124 offset:7552\n" \
  "ds_read_b64 v[86:87], v125 offset:7552\n" \
  "v_exp_f32 v24, v24\n" \
  "v_exp_f32 v25, v25\n" \
  "v_exp_f32 v26, v26\n" \
  "v_exp_f32 v27, v27\n" \
  "v_cvt_pk_bf16_f32 v104, v24, v25\n" \
  "v_cvt_pk_bf16_f32 v105, v26, v27\n" \
  "s_waitcnt lgkmcnt(4)\n" \
  "v_mfma_f32_32x32x16_bf16 v[48:63], v[92:95], v[100:103], v[48:63]\n" \
  "v_exp_f32 v28, v28\n" \
  "v_exp_f32 v29, v29\n" \
  "v_exp_f32 v30, v30\n" \
  "v_exp_f32 v31, v31\n" \
  "v_cvt_pk_bf16_f32 v106, v28, v29\n" \
  "v_cvt_pk_bf16_f32 v107, v30, v31\n" \
  "s_waitcnt lgkmcnt(2)\n" \
  "s_barrier\n" \
  "s_sub_u32 %[cnt], %[cnt], 1\n" \
  "s_cmp_lg_u32 %[cnt], 0\n" \
  "s_cbranch_scc1 1b\n" \
  "s_waitcnt lgkmcnt(1)\n" \
  "v_mfma_f32_32x32x16_bf16 v[16:31], v[88:91], v[80:83], 0\n" \
  "ds_read_b64_tr_b16 v[92:93], v126 offset:7552\n" \
  "ds_read_b64_tr_b16 v[94:95], v126 offset:7680\n" \
  "v_exp_f32 v32, v32\n" \
  "v_exp_f32 v33, v33\n" \
  "v_exp_f32 v34, v34\n" \
  "v_exp_f32 v35, v35\n" \
  "v_cvt_pk_bf16_f32 v100, v32, v33\n" \
  "v_cvt_pk_bf16_f32 v101, v34, v35\n" \
  "v_mfma_f32_32x32x16_bf16 v[48:63], v[96:99], v[104:107], v[48:63]\n" \
  "ds_read_b64_tr_b16 v[96:97], v126 offset:7808\n" \
  "ds_read_b64_tr_b16 v[98:99], v126 offset:7936\n" \
  "v_exp_f32 v36, v36\n" \
  "v_exp_f32 v37, v37\n" \
  "v_exp_f32 v38, v38\n" \
  "v_exp_f32 v39, v39\n" \
  "v_cvt_pk_bf16_f32 v102, v36, v37\n" \
  "v_cvt_pk_bf16_f32 v103, v38, v39\n" \
  "s_waitcnt lgkmcnt(4)\n" \
  "v_mfma_f32_32x32x8_bf16 v[16:31], v[86:87], v[84:85], v[16:31]\n" \
  "ds_read_b128 v[88:91], v124 offset:11328\n" \
  "ds_read_b64 v[86:87], v125 offset:11328\n" \
  "v_exp_f32 v40, v40\n" \
  "v_exp_f32 v41, v41\n" \
  "v_exp_f32 v42, v42\n" \
  "v_exp_f32 v43, v43\n" \
  "v_cvt_pk_bf16_f32 v104, v40, v41\n" \
  "v_cvt_pk_bf16_f32 v105, v42, v43\n" \
  "s_waitcnt lgkmcnt(4)\n" \
  "v_mfma_f32_32x32x16_bf16 v[48:63], v[92:95], v[100:103], v[48:63]\n" \
  "v_exp_f32 v44, v44\n" \
  "v_exp_f32 v45, v45\n" \
  "v_exp_f32 v46, v46\n" \
  "v_exp_f32 v47, v47\n" \
  "v_cvt_pk_bf16_f32 v106, v44, v45\n" \
  "v_cvt_pk_bf16_f32 v107, v46, v47\n" \
  "s_waitcnt lgkmcnt(1)\n" \
  "v_mfma_f32_32x32x16_bf16 v[32:47], v[88:91], v[80:83], 0\n" \
  "ds_read_b64_tr_b16 v[92:93], v126 offset:11328\n" \
  "ds_read_b64_tr_b16 v[94:95], v126 offset:11456\n" \
  "v_exp_f32 v16, v16\n" \
  "v_exp_f32 v17, v17\n" \
  "v_exp_f32 v18, v18\n" \
  "v_exp_f32 v19, v19\n" \
  "v_cvt_pk_bf16_f32 v100, v16, v17\n" \
  "v_cvt_pk_bf16_f32 v101, v18, v19\n" \
  "v_mfma_f32_32x32x16_bf16 v[48:63], v[96:99], v[104:107], v[48:63]\n" \
  "ds_read_b64_tr_b16 v[96:97], v126 offset:11584\n" \
  "ds_read_b64_tr_b16 v[98:99], v126 offset:11712\n" \
  "v_exp_f32 v20, v20\n" \
  "v_exp_f32 v21, v21\n" \
  "v_exp_f32 v22, v22\n" \
  "v_exp_f32 v23, v23\n" \
  "v_cvt_pk_bf16_f32 v102, v20, v21\n" \
  "v_cvt_pk_bf16_f32 v103, v22, v23\n" \
  "s_barrier\n" \
  "s_waitcnt lgkmcnt(4)\n" \
  "v_mfma_f32_32x32x8_bf16 v[32:47], v[86:87], v[84:85], v[32:47]\n" \
  "ds_read_b128 v[88:91], v124 offset:15104\n" \
  "ds_read_b64 v[86:87], v125 offset:15104\n" \
  "v_exp_f32 v24, v24\n" \
  "v_exp_f32 v25, v25\n" \
  "v_exp_f32 v26, v26\n" \
  "v_exp_f32 v27, v27\n" \
  "v_cvt_pk_bf16_f32 v104, v24, v25\n" \
  "v_cvt_pk_bf16_f32 v105, v26, v27\n" \
  "s_waitcnt lgkmcnt(4)\n" \
  "v_mfma_f32_32x32x16_bf16 v[48:63], v[92:95], v[100:103], v[48:63]\n" \
  "v_exp_f32 v28, v28\n" \
  "v_exp_f32 v29, v29\n" \
  "v_exp_f32 v30, v30\n" \
  "v_exp_f32 v31, v31\n" \
  "v_cvt_pk_bf16_f32 v106, v28, v29\n" \
  "v_cvt_pk_bf16_f32 v107, v30, v31\n" \
  "s_waitcnt lgkmcnt(1)\n" \
  "v_mfma_f32_32x32x16_bf16 v[16:31], v[88:91], v[80:83], 0\n" \
  "ds_read_b64_tr_b16 v[92:93], v126 offset:15104\n" \
  "ds_read_b64_tr_b16 v[94:95], v126 offset:15232\n" \
  "v_exp_f32 v32, v32\n" \
  "v_exp_f32 v33, v33\n" \
  "v_exp_f32 v34, v34\n" \
  "v_exp_f32 v35, v35\n" \
  "v_cvt_pk_bf16_f32 v100, v32, v33\n" \
  "v_cvt_pk_bf16_f32 v101, v34, v35\n" \
  "v_mfma_f32_32x32x16_bf16 v[48:63], v[96:99], v[104:107], v[48:63]\n" \
  "ds_read_b64_tr_b16 v[96:97], v126 offset:15360\n" \
  "ds_read_b64_tr_b16 v[98:99], v126 offset:15488\n" \
  "v_exp_f32 v36, v36\n" \
  "v_exp_f32 v37, v37\n" \
  "v_exp_f32 v38, v38\n" \
  "v_exp_f32 v39, v39\n" \
  "v_cvt_pk_bf16_f32 v102, v36, v37\n" \
  "v_cvt_pk_bf16_f32 v103, v38, v39\n" \
  "s_waitcnt lgkmcnt(4)\n" \
  "v_mfma_f32_32x32x8_bf16 v[16:31], v[86:87], v[84:85], v[16:31]\n" \
  "ds_read_b128 v[88:91], v124 offset:18880\n" \
  "ds_read_b64 v[86:87], v125 offset:18880\n" \
  "v_exp_f32 v40, v40\n" \
  "v_exp_f32 v41, v41\n" \
  "v_exp_f32 v42, v42\n" \
  "v_exp_f32 v43, v43\n" \
  "v_cvt_pk_bf16_f32 v104, v40, v41\n" \
  "v_cvt_pk_bf16_f32 v105, v42, v43\n" \
  "s_waitcnt lgkmcnt(4)\n" \
  "v_mfma_f32_32x32x16_bf16 v[48:63], v[92:95], v[100:103], v[48:63]\n" \
  "v_exp_f32 v44, v44\n" \
  "v_exp_f32 v45, v45\n" \
  "v_exp_f32 v46, v46\n" \
  "v_exp_f32 v47, v47\n" \
  "v_cvt_pk_bf16_f32 v106, v44, v45\n" \
  "v_cvt_pk_bf16_f32 v107, v46, v47\n" \
  "s_waitcnt lgkmcnt(0)\n" \
  "s_nop 7\n" \
  ""
#define MIX_B0 \
  "s_mov_b32 %[cnt], 8\n" \
  "1:\n" \
  "s_waitcnt lgkmcnt(1)\n" \
  "v_mfma_f32_32x32x16_bf16 v[32:47], v[88:91], v[80:83], 0\n" \
  "s_barrier\n" \
  "ds_read_b64_tr_b16 v[92:93], v126 offset:18880\n" \
  "ds_read_b64_tr_b16 v[94:95], v126 offset:19008\n" \
  "v_exp_f32 v16, v16\n" \
  "v_exp_f32 v17, v17\n" \
  "v_exp_f32 v18, v18\n" \
  "v_exp_f32 v19, v19\n" \
  "v_cvt_pk_bf16_f32 v100, v16, v17\n" \
  "v_cvt_pk_bf16_f32 v101, v18, v19\n" \
  "v_mfma_f32_32x32x16_bf16 v[48:63], v[96:99], v[104:107], v[48:63]\n" \
  "ds_read_b64_tr_b16 v[96:97], v126 offset:19136\n" \
  "ds_read_b64_tr_b16 v[98:99], v126 offset:19264\n" \
  "v_exp_f32 v20, v20\n" \
  "v_exp_f32 v21, v21\n" \
  "v_exp_f32 v22, v22\n" \
  "v_exp_f32 v23, v23\n" \
  "v_cvt_pk_bf16_f32 v102, v20, v21\n" \
  "v_cvt_pk_bf16_f32 v103, v22, v23\n" \
  "s_waitcnt lgkmcnt(4)\n" \
  "v_mfma_f32_32x32x8_bf16 v[32:47], v[86:87], v[84:85], v[32:47]\n" \
  "ds_read_b128 v[88:91], v124 offset:0\n" \
  "ds_read_b64 v[86:87], v125 offset:0\n" \
  "v_exp_f32 v24, v24\n" \
  "v_exp_f32 v25, v25\n" \
  "v_exp_f32 v26, v26\n" \
  "v_exp_f32 v27, v27\n" \
  "v_cvt_pk_bf16_f32 v104, v24, v25\n" \
  "v_cvt_pk_bf16_f32 v105, v26, v27\n" \
  "s_waitcnt lgkmcnt(4)\n" \
  "v_mfma_f32_32x32x16_bf16 v[48:63], v[92:95], v[100:103], v[48:63]\n" \
  "v_exp_f32 v28, v28\n" \
  "v_exp_f32 v29, v29\n" \
  "v_exp_f32 v30, v30\n" \
  "v_exp_f32 v31, v31\n" \
  "v_cvt_pk_bf16_f32 v106, v28, v29\n" \
  "v_cvt_pk_bf16_f32 v107, v30, v31\n" \
  "s_waitcnt lgkmcnt(1)\n" \
  "v_mfma_f32_32x32x16_bf16 v[16:31], v[88:91], v[80:83], 0\n" \
  "ds_read_b64_tr_b16 v[92:93], v126 offset:0\n" \
  "ds_read_b64_tr_b16 v[94:95], v126 offset:128\n" \
  "v_exp_f32 v32, v32\n" \
  "v_exp_f32 v33, v33\n" \
  "v_exp_f32 v34, v34\n" \
  "v_exp_f32 v35, v35\n" \
  "v_cvt_pk_bf16_f32 v100, v32, v33\n" \
  "v_cvt_pk_bf16_f32 v101, v34, v35\n" \
  "v_mfma_f32_32x32x16_bf16 v[48:63], v[96:99], v[104:107], v[48:63]\n" \
  "ds_read_b64_tr_b16 v[96:97], v126 offset:256\n" \
  "ds_read_b64_tr_b16 v[98:99], v126 offset:384\n" \
  "v_exp_f32 v36, v36\n" \
  "v_exp_f32 v37, v37\n" \
  "v_exp_f32 v38, v38\n" \
  "v_exp_f32 v39, v39\n" \
  "v_cvt_pk_bf16_f32 v102, v36, v37\n" \
  "v_cvt_pk_bf16_f32 v103, v38, v39\n" \
  "s_waitcnt lgkmcnt(4)\n" \
  "v_mfma_f32_32x32x8_bf16 v[16:31], v[86:87], v[84:85], v[16:31]\n" \
  "s_barrier\n" \
  "ds_read_b128 v[88:91], v124 offset:3776\n" \
  "ds_read_b64 v[86:87], v125 offset:3776\n" \
  "v_exp_f32 v40, v40\n" \
  "v_exp_f32 v41, v41\n" \
  "v_exp_f32 v42, v42\n" \
  "v_exp_f32 v43, v43\n" \
  "v_cvt_pk_bf16_f32 v104, v40, v41\n" \
  "v_cvt_pk_bf16_f32 v105, v42, v43\n" \
  "s_waitcnt lgkmcnt(4)\n" \
  "v_mfma_f32_32x32x16_bf16 v[48:63], v[92:95], v[100:103], v[48:63]\n" \
  "v_exp_f32 v44, v44\n" \
  "v_exp_f32 v45, v45\n" \
  "v_exp_f32 v46, v46\n" \
  "v_exp_f32 v47, v47\n" \
  "v_cvt_pk_bf16_f32 v106, v44, v45\n" \
  "v_cvt_pk_bf16_f32 v107, v46, v47\n" \
  "s_waitcnt lgkmcnt(1)\n" \
  "v_mfma_f32_32x32x16_bf16 v[32:47], v[88:91], v[80:83], 0\n" \
  "ds_read_b64_tr_b16 v[92:93], v126 offset:3776\n" \
  "ds_read_b64_tr_b16 v[94:95], v126 offset:3904\n" \
  "v_exp_f32 v16, v16\n" \
  "v_exp_f32 v17, v17\n" \
  "v_exp_f32 v18, v18\n" \
  "v_exp_f32 v19, v19\n" \
  "v_cvt_pk_bf16_f32 v100, v16, v17\n" \
  "v_cvt_pk_bf16_f32 v101, v18, v19\n" \
  "v_mfma_f32_32x32x16_bf16 v[48:63], v[96:99], v[104:107], v[48:63]\n" \
  "ds_read_b64_tr_b16 v[96:97], v126 offset:4032\n" \
  "ds_read_b64_tr_b16 v[98:99], v126 offset:4160\n" \
  "v_exp_f32 v20, v20\n" \
  "v_exp_f32 v21, v21\n" \
  "v_exp_f32 v22, v22\n" \
  "v_exp_f32 v23, v23\n" \
  "v_cvt_pk_bf16_f32 v102, v20, v21\n" \
  "v_cvt_pk_bf16_f32 v103, v22, v23\n" \
  "s_waitcnt lgkmcnt(4)\n" \
  "v_mfma_f32_32x32x8_bf16 v[32:47], v[86:87], v[84:85], v[32:47]\n" \
  "ds_read_b128 v[88:91], v124 offset:7552\n" \
  "ds_read_b64 v[86:87], v125 offset:7552\n" \
  "v_exp_f32 v24, v24\n" \
  "v_exp_f32 v25, v25\n" \
  "v_exp_f32 v26, v26\n" \
  "v_exp_f32 v27, v27\n" \
  "v_cvt_pk_bf16_f32 v104, v24, v25\n" \
  "v_cvt_pk_bf16_f32 v105, v26, v27\n" \
  "s_waitcnt lgkmcnt(4)\n" \
  "v_mfma_f32_32x32x16_bf16 v[48:63], v[92:95], v[100:103], v[48:63]\n" \
  "v_exp_f32 v28, v28\n" \
  "v_exp_f32 v29, v29\n" \
  "v_exp_f32 v30, v30\n" \
  "v_exp_f32 v31, v31\n" \
  "v_cvt_pk_bf16_f32 v106, v28, v29\n" \
  "v_cvt_pk_bf16_f32 v107, v30, v31\n" \
  "s_waitcnt lgkmcnt(1)\n" \
  "v_mfma_f32_32x32x16_bf16 v[16:31], v[88:91], v[80:83], 0\n" \
  "s_barrier\n" \
  "ds_read_b64_tr_b16 v[92:93], v126 offset:7552\n" \
  "ds_read_b64_tr_b16 v[94:95], v126 offset:7680\n" \
  "v_exp_f32 v32, v32\n" \
  "v_exp_f32 v33, v33\n" \
  "v_exp_f32 v34, v34\n" \
  "v_exp_f32 v35, v35\n" \
  "v_cvt_pk_bf16_f32 v100, v32, v33\n" \
  "v_cvt_pk_bf16_f32 v101, v34, v35\n" \
  "v_mfma_f32_32x32x16_bf16 v[48:63], v[96:99], v[104:107], v[48:63]\n" \
  "ds_read_b64_tr_b16 v[96:97], v126 offset:7808\n" \
  "ds_read_b64_tr_b16 v[98:99], v126 offset:7936\n" \
  "v_exp_f32 v36, v36\n" \
  "v_exp_f32 v37, v37\n" \
  "v_exp_f32 v38, v38\n" \
  "v_exp_f32 v39, v39\n" \
  "v_cvt_pk_bf16_f32 v102, v36, v37\n" \
  "v_cvt_pk_bf16_f32 v103, v38, v39\n" \
  "s_waitcnt lgkmcnt(4)\n" \
  "v_mfma_f32_32x32x8_bf16 v[16:31], v[86:87], v[84:85], v[16:31]\n" \
  "ds_read_b128 v[88:91], v124 offset:11328\n" \
  "ds_read_b64 v[86:87], v125 offset:11328\n" \
  "v_exp_f32 v40, v40\n" \
  "v_exp_f32 v41, v41\n" \
  "v_exp_f32 v42, v42\n" \
  "v_exp_f32 v43, v43\n" \
  "v_cvt_pk_bf16_f32 v104, v40, v41\n" \
  "v_cvt_pk_bf16_f32 v105, v42, v43\n" \
  "s_waitcnt lgkmcnt(4)\n" \
  "v_mfma_f32_32x32x16_bf16 v[48:63], v[92:95], v[100:103], v[48:63]\n" \
  "v_exp_f32 v44, v44\n" \
  "v_exp_f32 v45, v45\n" \
  "v_exp_f32 v46, v46\n" \
  "v_exp_f32 v47, v47\n" \
  "v_cvt_pk_bf16_f32 v106, v44, v45\n" \
  "v_cvt_pk_bf16_f32 v107, v46, v47\n" \
  "s_waitcnt lgkmcnt(1)\n" \
  "v_mfma_f32_32x32x16_bf16 v[32:47], v[88:91], v[80:83], 0\n" \
  "ds_read_b64_tr_b16 v[92:93], v126 offset:11328\n" \
  "ds_read_b64_tr_b16 v[94:95], v126 offset:11456\n" \
  "v_exp_f32 v16, v16\n" \
  "v_exp_f32 v17, v17\n" \
  "v_exp_f32 v18, v18\n" \
  "v_exp_f32 v19, v19\n" \
  "v_cvt_pk_bf16_f32 v100, v16, v17\n" \
  "v_cvt_pk_bf16_f32 v101, v18, v19\n" \
  "v_mfma_f32_32x32x16_bf16 v[48:63], v[96:99], v[104:107], v[48:63]\n" \
  "ds_read_b64_tr_b16 v[96:97], v126 offset:11584\n" \
  "ds_read_b64_tr_b16 v[98:99], v126 offset:11712\n" \
  "v_exp_f32 v20, v20\n" \
  "v_exp_f32 v21, v21\n" \
  "v_exp_f32 v22, v22\n" \
  "v_exp_f32 v23, v23\n" \
  "v_cvt_pk_bf16_f32 v102, v20, v21\n" \
  "v_cvt_pk_bf16_f32 v103, v22, v23\n" \
  "s_waitcnt lgkmcnt(4)\n" \
  "v_mfma_f32_32x32x8_bf16 v[32:47], v[86:87], v[84:85], v[32:47]\n" \
  "s_barrier\n" \
  "ds_read_b128 v[88:91], v124 offset:15104\n" \
  "ds_read_b64 v[86:87], v125 offset:15104\n" \
  "v_exp_f32 v24, v24\n" \
  "v_exp_f32 v25, v25\n" \
  "v_exp_f32 v26, v26\n" \
  "v_exp_f32 v27, v27\n" \
  "v_cvt_pk_bf16_f32 v104, v24, v25\n" \
  "v_cvt_pk_bf16_f32 v105, v26, v27\n" \
  "s_waitcnt lgkmcnt(4)\n" \
  "v_mfma_f32_32x32x16_bf16 v[48:63], v[92:95], v[100:103], v[48:63]\n" \
  "v_exp_f32 v28, v28\n" \
  "v_exp_f32 v29, v29\n" \
  "v_exp_f32 v30, v30\n" \
  "v_exp_f32 v31, v31\n" \
  "v_cvt_pk_bf16_f32 v106, v28, v29\n" \
  "v_cvt_pk_bf16_f32 v107, v30, v31\n" \
  "s_waitcnt lgkmcnt(1)\n" \
  "v_mfma_f32_32x32x16_bf16 v[16:31], v[88:91], v[80:83], 0\n" \
  "ds_read_b64_tr_b16 v[92:93], v126 offset:15104\n" \
  "ds_read_b64_tr_b16 v[94:95], v126 offset:15232\n" \
  "v_exp_f32 v32, v32\n" \
  "v_exp_f32 v33, v33\n" \
  "v_exp_f32 v34, v34\n" \
  "v_exp_f32 v35, v35\n" \
  "v_cvt_pk_bf16_f32 v100, v32, v33\n" \
  "v_cvt_pk_bf16_f32 v101, v34, v35\n" \
  "v_mfma_f32_32x32x16_bf16 v[48:63], v[96:99], v[104:107], v[48:63]\n" \
  "ds_read_b64_tr_b16 v[96:97], v126 offset:15360\n" \
  "ds_read_b64_tr_b16 v[98:99], v126 offset:15488\n" \
  "v_exp_f32 v36, v36\n" \
  "v_exp_f32 v37, v37\n" \
  "v_exp_f32 v38, v38\n" \
  "v_exp_f32 v39, v39\n" \
  "v_cvt_pk_bf16_f32 v102, v36, v37\n" \
  "v_cvt_pk_bf16_f32 v103, v38, v39\n" \
  "s_waitcnt lgkmcnt(4)\n" \
  "v_mfma_f32_32x32x8_bf16 v[16:31], v[86:87], v[84:85], v[16:31]\n" \
  "ds_read_b128 v[88:91], v124 offset:18880\n" \
  "ds_read_b64 v[86:87], v125 offset:18880\n" \
  "v_exp_f32 v40, v40\n" \
  "v_exp_f32 v41, v41\n" \
  "v_exp_f32 v42, v42\n" \
  "v_exp_f32 v43, v43\n" \
  "v_cvt_pk_bf16_f32 v104, v40, v41\n" \
  "v_cvt_pk_bf16_f32 v105, v42, v43\n" \
  "s_waitcnt lgkmcnt(4)\n" \
  "v_mfma_f32_32x32x16_bf16 v[48:63], v[92:95], v[100:103], v[48:63]\n" \
  "v_exp_f32 v44, v44\n" \
  "v_exp_f32 v45, v45\n" \
  "v_exp_f32 v46, v46\n" \
  "v_exp_f32 v47, v47\n" \
  "v_cvt_pk_bf16_f32 v106, v44, v45\n" \
  "v_cvt_pk_bf16_f32 v107, v46, v47\n" \
  "s_sub_u32 %[cnt], %[cnt], 1\n" \
  "s_cmp_lg_u32 %[cnt], 0\n" \
  "s_cbranch_scc1 1b\n" \
  "s_waitcnt lgkmcnt(2)\n" \
  "s_nop 0\n" \
  "v_mfma_f32_32x32x16_bf16 v[48:63], v[96:99], v[104:107], v[48:63]\n" \
  "s_waitcnt lgkmcnt(0)\n" \
  "s_barrier\n" \
  "s_nop 15\n" \
  ""

DI f32x16 att_fallback(bf16x8 qa, s16x4 qc, char* wl, const char* qkv, int oa, int ob, int oc, int hoff, int l) {
  const int r = l & 31, h = l >> 5;
  __builtin_amdgcn_fence(__ATOMIC_ACQUIRE, "agent");
  for (int i = l; i < 128; i += 64) *(LAS unsigned*)((LAS char*)wl + KV_ONES + i * 4) = 0x3F803F80u;
  const int q = (l >> 2) & 3, p = l & 3;
  const lptr pv = (lptr)wl + ((l & 16) ? (p == 0 ? 2 * 1088 + 512 + hoff : KV_ONES) : (p & 1) * 1088 + 512 + (p >> 1) * 8) + h * 64 + q * 16;
  f32x16 o = {};
  float mx = -3.0e38f;
  for (int t = 0; t < 64; ++t) {
    const unsigned so = (t * 32 + r) * 16;
    const bf16x8 ka = *(const bf16x8*)(qkv + (size_t)(80 + (h ? ob : oa)) * PLANE + so);
    const s16x4 kc = *(const s16x4*)(qkv + (size_t)(80 + oc) * PLANE + so + hoff);
    const u32x4 va = *(const u32x4*)(qkv + (size_t)(160 + (h ? ob : oa)) * PLANE + so);
    const u32x4 vc = *(const u32x4*)(qkv + (size_t)(160 + oc) * PLANE + so);
    *(LAS u32x4*)((LAS char*)wl + h * 1088 + 512 + r * 16) = va;
    *(LAS u32x4*)((LAS char*)wl + 2 * 1088 + 512 + r * 16) = vc;
    asm volatile("s_waitcnt lgkmcnt(0)" ::: "memory");
    const f32x16 z = {};
    f32x16 s = mfma8(kc, qc, mfma16(ka, qa, z));
    float tm = s[0];
#pragma unroll
    for (int i = 1; i < 16; ++i) tm = __builtin_fmaxf(tm, s[i]);
    tm = __builtin_fmaxf(tm, __shfl_xor(tm, 32));
    const float mn = __builtin_fmaxf(mx, tm), al = __builtin_amdgcn_exp2f(mx - mn);
    mx = mn;
#pragma unroll
    for (int i = 0; i < 16; ++i) { s[i] = __builtin_amdgcn_exp2f(s[i] - mn); o[i] *= al; }
    const bf16x8 v0 = ldtr8(pv, 128), v1 = ldtr8(pv + 256, 128);
    o = mfma16(v0, __builtin_bit_cast(bf16x8, pk8(s, 0)), o);
    o = mfma16(v1, __builtin_bit_cast(bf16x8, pk8(s, 8)), o);
    asm volatile("s_waitcnt lgkmcnt(0)" ::: "memory");
  }
  return o;
}

#define OUT_LOOP3 \
  "s_mov_b32 %[sa], 0\n" \
  "s_mov_b32 %[sw], 0\n" \
  "s_mov_b32 %[st], 11\n" \
  "v_mov_b32 v98, v96\n" \
  "v_mov_b32 v99, v97\n" \
  "s_add_u32 %[sa], %[sa], 0x2000\n" \
  "s_cmp_eq_u32 %[sa], 0xe000\n" \
  "s_cselect_b32 %[sa], 0, %[sa]\n" \
  "s_add_u32 %[sw], %[sw], 0x2800\n" \
  "s_cmp_eq_u32 %[sw], 0x19000\n" \
  "s_cselect_b32 %[sw], 0, %[sw]\n" \
  "v_add_u32 v100, %[sa], v96\n" \
  "v_add_u32 v101, %[sw], v97\n" \
  "v_mov_b64 v[16:17], 0\n" \
  "v_mov_b64 v[18:19], 0\n" \
  "v_mov_b64 v[20:21], 0\n" \
  "v_mov_b64 v[22:23], 0\n" \
  "v_mov_b64 v[24:25], 0\n" \
  "v_mov_b64 v[26:27], 0\n" \
  "s_barrier\n" \
  "ds_read_b128 v[32:35], v98 offset:0\n" \
  "ds_read_b128 v[36:39], v99 offset:0\n" \
  "ds_read_b128 v[40:43], v99 offset:256\n" \
  "ds_read_b128 v[44:47], v99 offset:512\n" \
  "ds_read_b128 v[48:51], v98 offset:4096\n" \
  "ds_read_b128 v[52:55], v99 offset:5120\n" \
  "ds_read_b128 v[56:59], v99 offset:5376\n" \
  "ds_read_b128 v[60:63], v99 offset:5632\n" \
  "1:\n" \
  "s_waitcnt lgkmcnt(6)\n" \
  "v_mfma_f32_16x16x32_bf16 v[16:19], v[36:39], v[32:35], v[16:19]\n" \
  "ds_read_b128 v[64:67], v100 offset:0\n" \
  "s_waitcnt lgkmcnt(6)\n" \
  "v_mfma_f32_16x16x32_bf16 v[20:23], v[40:43], v[32:35], v[20:23]\n" \
  "ds_read_b128 v[68:71], v101 offset:0\n" \
  "s_add_u32 %[sa], %[sa], 0x2000\n" \
  "s_cmp_eq_u32 %[sa], 0xe000\n" \
  "s_cselect_b32 %[sa], 0, %[sa]\n" \
  "s_waitcnt lgkmcnt(6)\n" \
  "v_mfma_f32_16x16x32_bf16 v[24:27], v[44:47], v[32:35], v[24:27]\n" \
  "ds_read_b128 v[72:75], v101 offset:256\n" \
  "ds_read_b128 v[76:79], v101 offset:512\n" \
  "s_add_u32 %[sw], %[sw], 0x2800\n" \
  "s_cmp_eq_u32 %[sw], 0x19000\n" \
  "s_cselect_b32 %[sw], 0, %[sw]\n" \
  "s_waitcnt lgkmcnt(6)\n" \
  "v_mfma_f32_16x16x32_bf16 v[16:19], v[52:55], v[48:51], v[16:19]\n" \
  "ds_read_b128 v[80:83], v100 offset:4096\n" \
  "v_add_u32 v98, %[sa], v96\n" \
  "v_add_u32 v99, %[sw], v97\n" \
  "s_waitcnt lgkmcnt(6)\n" \
  "v_mfma_f32_16x16x32_bf16 v[20:23], v[56:59], v[48:51], v[20:23]\n" \
  "ds_read_b128 v[84:87], v101 offset:5120\n" \
  "s_waitcnt lgkmcnt(6)\n" \
  "v_mfma_f32_16x16x32_bf16 v[24:27], v[60:63], v[48:51], v[24:27]\n" \
  "ds_read_b128 v[88:91], v101 offset:5376\n" \
  "ds_read_b128 v[92:95], v101 offset:5632\n" \
  "s_barrier\n" \
  "s_waitcnt lgkmcnt(6)\n" \
  "v_mfma_f32_16x16x32_bf16 v[16:19], v[68:71], v[64:67], v[16:19]\n" \
  "ds_read_b128 v[32:35], v98 offset:0\n" \
  "s_waitcnt lgkmcnt(6)\n" \
  "v_mfma_f32_16x16x32_bf16 v[20:23], v[72:75], v[64:67], v[20:23]\n" \
  "ds_read_b128 v[36:39], v99 offset:0\n" \
  "s_add_u32 %[sa], %[sa], 0x2000\n" \
  "s_cmp_eq_u32 %[sa], 0xe000\n" \
  "s_cselect_b32 %[sa], 0, %[sa]\n" \
  "s_waitcnt lgkmcnt(6)\n" \
  "v_mfma_f32_16x16x32_bf16 v[24:27], v[76:79], v[64:67], v[24:27]\n" \
  "ds_read_b128 v[40:43], v99 offset:256\n" \
  "ds_read_b128 v[44:47], v99 offset:512\n" \
  "s_add_u32 %[sw], %[sw], 0x2800\n" \
  "s_cmp_eq_u32 %[sw], 0x19000\n" \
  "s_cselect_b32 %[sw], 0, %[sw]\n" \
  "s_waitcnt lgkmcnt(6)\n" \
  "v_mfma_f32_16x16x32_bf16 v[16:19], v[84:87], v[80:83], v[16:19]\n" \
  "ds_read_b128 v[48:51], v98 offset:4096\n" \
  "v_add_u32 v100, %[sa], v96\n" \
  "v_add_u32 v101, %[sw], v97\n" \
  "s_waitcnt lgkmcnt(6)\n" \
  "v_mfma_f32_16x16x32_bf16 v[20:23], v[88:91], v[80:83], v[20:23]\n" \
  "ds_read_b128 v[52:55], v99 offset:5120\n" \
  "s_waitcnt lgkmcnt(6)\n" \
  "v_mfma_f32_16x16x32_bf16 v[24:27], v[92:95], v[80:83], v[24:27]\n" \
  "ds_read_b128 v[56:59], v99 offset:5376\n" \
  "ds_read_b128 v[60:63], v99 offset:5632\n" \
  "s_sub_u32 %[st], %[st], 1\n" \
  "s_cmp_lg_u32 %[st], 0\n" \
  "s_cbranch_scc1 1b\n" \
  "s_waitcnt lgkmcnt(6)\n" \
  "v_mfma_f32_16x16x32_bf16 v[16:19], v[36:39], v[32:35], v[16:19]\n" \
  "ds_read_b128 v[64:67], v100 offset:0\n" \
  "s_waitcnt lgkmcnt(6)\n" \
  "v_mfma_f32_16x16x32_bf16 v[20:23], v[40:43], v[32:35], v[20:23]\n" \
  "ds_read_b128 v[68:71], v101 offset:0\n" \
  "s_add_u32 %[sa], %[sa], 0x2000\n" \
  "s_cmp_eq_u32 %[sa], 0xe000\n" \
  "s_cselect_b32 %[sa], 0, %[sa]\n" \
  "s_waitcnt lgkmcnt(6)\n" \
  "v_mfma_f32_16x16x32_bf16 v[24:27], v[44:47], v[32:35], v[24:27]\n" \
  "ds_read_b128 v[72:75], v101 offset:256\n" \
  "ds_read_b128 v[76:79], v101 offset:512\n" \
  "s_add_u32 %[sw], %[sw], 0x2800\n" \
  "s_cmp_eq_u32 %[sw], 0x19000\n" \
  "s_cselect_b32 %[sw], 0, %[sw]\n" \
  "s_waitcnt lgkmcnt(6)\n" \
  "v_mfma_f32_16x16x32_bf16 v[16:19], v[52:55], v[48:51], v[16:19]\n" \
  "ds_read_b128 v[80:83], v100 offset:4096\n" \
  "v_add_u32 v98, %[sa], v96\n" \
  "v_add_u32 v99, %[sw], v97\n" \
  "s_waitcnt lgkmcnt(6)\n" \
  "v_mfma_f32_16x16x32_bf16 v[20:23], v[56:59], v[48:51], v[20:23]\n" \
  "ds_read_b128 v[84:87], v101 offset:5120\n" \
  "s_waitcnt lgkmcnt(6)\n" \
  "v_mfma_f32_16x16x32_bf16 v[24:27], v[60:63], v[48:51], v[24:27]\n" \
  "ds_read_b128 v[88:91], v101 offset:5376\n" \
  "ds_read_b128 v[92:95], v101 offset:5632\n" \
  "s_waitcnt lgkmcnt(6)\n" \
  "v_mfma_f32_16x16x32_bf16 v[16:19], v[68:71], v[64:67], v[16:19]\n" \
  "s_waitcnt lgkmcnt(5)\n" \
  "v_mfma_f32_16x16x32_bf16 v[20:23], v[72:75], v[64:67], v[20:23]\n" \
  "s_waitcnt lgkmcnt(4)\n" \
  "v_mfma_f32_16x16x32_bf16 v[24:27], v[76:79], v[64:67], v[24:27]\n" \
  "s_waitcnt lgkmcnt(2)\n" \
  "v_mfma_f32_16x16x32_bf16 v[16:19], v[84:87], v[80:83], v[16:19]\n" \
  "s_waitcnt lgkmcnt(1)\n" \
  "v_mfma_f32_16x16x32_bf16 v[20:23], v[88:91], v[80:83], v[20:23]\n" \
  "s_waitcnt lgkmcnt(0)\n" \
  "v_mfma_f32_16x16x32_bf16 v[24:27], v[92:95], v[80:83], v[24:27]\n" \
  "s_nop 7\n" \
  ""
#define OUT_LOOP2 \
  "s_mov_b32 %[sa], 0\n" \
  "s_mov_b32 %[sw], 0\n" \
  "s_mov_b32 %[st], 11\n" \
  "v_mov_b32 v98, v96\n" \
  "v_mov_b32 v99, v97\n" \
  "s_add_u32 %[sa], %[sa], 0x2000\n" \
  "s_cmp_eq_u32 %[sa], 0xe000\n" \
  "s_cselect_b32 %[sa], 0, %[sa]\n" \
  "s_add_u32 %[sw], %[sw], 0x2800\n" \
  "s_cmp_eq_u32 %[sw], 0x19000\n" \
  "s_cselect_b32 %[sw], 0, %[sw]\n" \
  "v_add_u32 v100, %[sa], v96\n" \
  "v_add_u32 v101, %[sw], v97\n" \
  "v_mov_b64 v[16:17], 0\n" \
  "v_mov_b64 v[18:19], 0\n" \
  "v_mov_b64 v[20:21], 0\n" \
  "v_mov_b64 v[22:23], 0\n" \
  "s_barrier\n" \
  "ds_read_b128 v[32:35], v98 offset:0\n" \
  "ds_read_b128 v[36:39], v99 offset:0\n" \
  "ds_read_b128 v[40:43], v99 offset:256\n" \
  "ds_read_b128 v[48:51], v98 offset:4096\n" \
  "ds_read_b128 v[52:55], v99 offset:5120\n" \
  "ds_read_b128 v[56:59], v99 offset:5376\n" \
  "1:\n" \
  "s_waitcnt lgkmcnt(4)\n" \
  "v_mfma_f32_16x16x32_bf16 v[16:19], v[36:39], v[32:35], v[16:19]\n" \
  "ds_read_b128 v[64:67], v100 offset:0\n" \
  "s_waitcnt lgkmcnt(4)\n" \
  "v_mfma_f32_16x16x32_bf16 v[20:23], v[40:43], v[32:35], v[20:23]\n" \
  "ds_read_b128 v[68:71], v101 offset:0\n" \
  "ds_read_b128 v[72:75], v101 offset:256\n" \
  "s_add_u32 %[sa], %[sa], 0x2000\n" \
  "s_cmp_eq_u32 %[sa], 0xe000\n" \
  "s_cselect_b32 %[sa], 0, %[sa]\n" \
  "s_waitcnt lgkmcnt(4)\n" \
  "v_mfma_f32_16x16x32_bf16 v[16:19], v[52:55], v[48:51], v[16:19]\n" \
  "ds_read_b128 v[80:83], v100 offset:4096\n" \
  "s_add_u32 %[sw], %[sw], 0x2800\n" \
  "s_cmp_eq_u32 %[sw], 0x19000\n" \
  "s_cselect_b32 %[sw], 0, %[sw]\n" \
  "s_waitcnt lgkmcnt(4)\n" \
  "v_mfma_f32_16x16x32_bf16 v[20:23], v[56:59], v[48:51], v[20:23]\n" \
  "ds_read_b128 v[84:87], v101 offset:5120\n" \
  "ds_read_b128 v[88:91], v101 offset:5376\n" \
  "v_add_u32 v98, %[sa], v96\n" \
  "v_add_u32 v99, %[sw], v97\n" \
  "s_barrier\n" \
  "s_waitcnt lgkmcnt(4)\n" \
  "v_mfma_f32_16x16x32_bf16 v[16:19], v[68:71], v[64:67], v[16:19]\n" \
  "ds_read_b128 v[32:35], v98 offset:0\n" \
  "s_waitcnt lgkmcnt(4)\n" \
  "v_mfma_f32_16x16x32_bf16 v[20:23], v[72:75], v[64:67], v[20:23]\n" \
  "ds_read_b128 v[36:39], v99 offset:0\n" \
  "ds_read_b128 v[40:43], v99 offset:256\n" \
  "s_add_u32 %[sa], %[sa], 0x2000\n" \
  "s_cmp_eq_u32 %[sa], 0xe000\n" \
  "s_cselect_b32 %[sa], 0, %[sa]\n" \
  "s_waitcnt lgkmcnt(4)\n" \
  "v_mfma_f32_16x16x32_bf16 v[16:19], v[84:87], v[80:83], v[16:19]\n" \
  "ds_read_b128 v[48:51], v98 offset:4096\n" \
  "s_add_u32 %[sw], %[sw], 0x2800\n" \
  "s_cmp_eq_u32 %[sw], 0x19000\n" \
  "s_cselect_b32 %[sw], 0, %[sw]\n" \
  "s_waitcnt lgkmcnt(4)\n" \
  "v_mfma_f32_16x16x32_bf16 v[20:23], v[88:91], v[80:83], v[20:23]\n" \
  "ds_read_b128 v[52:55], v99 offset:5120\n" \
  "ds_read_b128 v[56:59], v99 offset:5376\n" \
  "v_add_u32 v100, %[sa], v96\n" \
  "v_add_u32 v101, %[sw], v97\n" \
  "s_sub_u32 %[st], %[st], 1\n" \
  "s_cmp_lg_u32 %[st], 0\n" \
  "s_cbranch_scc1 1b\n" \
  "s_waitcnt lgkmcnt(4)\n" \
  "v_mfma_f32_16x16x32_bf16 v[16:19], v[36:39], v[32:35], v[16:19]\n" \
  "ds_read_b128 v[64:67], v100 offset:0\n" \
  "s_waitcnt lgkmcnt(4)\n" \
  "v_mfma_f32_16x16x32_bf16 v[20:23], v[40:43], v[32:35], v[20:23]\n" \
  "ds_read_b128 v[68:71], v101 offset:0\n" \
  "ds_read_b128 v[72:75], v101 offset:256\n" \
  "s_add_u32 %[sa], %[sa], 0x2000\n" \
  "s_cmp_eq_u32 %[sa], 0xe000\n" \
  "s_cselect_b32 %[sa], 0, %[sa]\n" \
  "s_waitcnt lgkmcnt(4)\n" \
  "v_mfma_f32_16x16x32_bf16 v[16:19], v[52:55], v[48:51], v[16:19]\n" \
  "ds_read_b128 v[80:83], v100 offset:4096\n" \
  "s_add_u32 %[sw], %[sw], 0x2800\n" \
  "s_cmp_eq_u32 %[sw], 0x19000\n" \
  "s_cselect_b32 %[sw], 0, %[sw]\n" \
  "s_waitcnt lgkmcnt(4)\n" \
  "v_mfma_f32_16x16x32_bf16 v[20:23], v[56:59], v[48:51], v[20:23]\n" \
  "ds_read_b128 v[84:87], v101 offset:5120\n" \
  "ds_read_b128 v[88:91], v101 offset:5376\n" \
  "v_add_u32 v98, %[sa], v96\n" \
  "v_add_u32 v99, %[sw], v97\n" \
  "s_waitcnt lgkmcnt(4)\n" \
  "v_mfma_f32_16x16x32_bf16 v[16:19], v[68:71], v[64:67], v[16:19]\n" \
  "s_waitcnt lgkmcnt(3)\n" \
  "v_mfma_f32_16x16x32_bf16 v[20:23], v[72:75], v[64:67], v[20:23]\n" \
  "s_waitcnt lgkmcnt(1)\n" \
  "v_mfma_f32_16x16x32_bf16 v[16:19], v[84:87], v[80:83], v[16:19]\n" \
  "s_waitcnt lgkmcnt(0)\n" \
  "v_mfma_f32_16x16x32_bf16 v[20:23], v[88:91], v[80:83], v[20:23]\n" \
  "s_nop 7\n" \
  ""
template <int NJ>
DI void out_compute(char* lds, char* ws, void* out, int w, int mt, int nt) {
  const int l = threadIdx.x & 63, tq = w & 3, g = l >> 4, i16 = l & 15;
  const unsigned lb = (unsigned)(size_t)lds;
  u32x2 ad = {lb + g * 1024 + (16 * tq + i16) * 16, lb + O_W + g * 1280 + (NJ == 3 ? 0 : 768) + i16 * 16};
  f32x4 c[3];
  const int slot = 16 * tq + i16;
  const unsigned oo = (mt * 64 + (slot & 32) + pi32(slot & 31)) * 1280 + nt * 160 + (NJ == 3 ? 0 : 96);
  const rsrc_t ro = mk_rsrc(out, S * D * 2);
  asm volatile("" : "+{v[96:97]}"(ad));
  bar();
  unsigned sa, sw, cn;
#define OUT_RUN(T) asm volatile(T : "={v[16:19]}"(c[0]), "={v[20:23]}"(c[1]), "={v[24:27]}"(c[2]), [sa] "=&s"(sa), [sw] "=&s"(sw), [st] "=&s"(cn) : "{v[96:97]}"(ad) \
      : "v32", "v33", "v34", "v35", "v36", "v37", "v38", "v39", C8(4), C8(5), C8(6), C8(7), C8(8), "v90", "v91", "v92", "v93", "v94", "v95", "v98", "v99", "v100", "v101", "scc", "memory")
  if (NJ == 3) OUT_RUN(OUT_LOOP3); else OUT_RUN(OUT_LOOP2);
  const auto s0 = __builtin_amdgcn_permlane16_swap(pk2(c[0][0], c[0][1]), pk2(c[1][0], c[1][1]), false, false);
  const auto s1 = __builtin_amdgcn_permlane16_swap(pk2(c[0][2], c[0][3]), pk2(c[1][2], c[1][3]), false, false);
  __builtin_amdgcn_raw_buffer_store_b128(u32x4{s0[0], s1[0], s0[1], s1[1]}, ro, oo + (((g & 1) << 1) | (g >> 1)) * 16, 0, 0);
  if (NJ == 3) __builtin_amdgcn_raw_buffer_store_b64(u32x2{pk2(c[NJ - 1][0], c[NJ - 1][1]), pk2(c[NJ - 1][2], c[NJ - 1][3])}, ro, oo + 64 + g * 8, 0, 0);
}

#define MIX_TEMPS "v92", "v93", "v94", "v95", "v102", "v103", "v108", "v109", "v110", "v111", "v112", "v113", "v114", "v115", \
                  "v116", "v117", "v118", "v119", "v120", "v121", "v122", "v123", "scc", "memory"
#define MIX_SY "v32", "v33", "v34", "v35", "v36", "v37", "v38", "v39", "v40", "v41", "v42", "v43", "v44", "v45", "v46", "v47"
#define MIX_Q "{v[80:83]}"(qa), "{v[84:85]}"(qc)
#define MIX_IN "{v124}"(ak), "{v125}"(akc), "{v126}"(av), "{v127}"(af), "{v[128:131]}"(at)
#define MIX_RUN_A(T) asm volatile(T : "={v[16:31]}"(sx), "={v[48:63]}"(o), "={v[86:87]}"(kc), "={v[88:91]}"(ka), "={v[96:99]}"(v1), \
                                      "={v[104:107]}"(p1), "={v[80:83]}"(qa), "={v[84:85]}"(qc), "+{v[100:101]}"(aq), [cnt] "=&s"(cnt) : MIX_IN : MIX_SY, MIX_TEMPS)
#define MIX_RUN_B0(T) asm volatile(T : "+{v[16:31]}"(sx), "+{v[48:63]}"(o), "+{v[86:87]}"(kc), "+{v[88:91]}"(ka), "+{v[96:99]}"(v1), \
                                      "+{v[104:107]}"(p1), [cnt] "=&s"(cnt) : MIX_Q, MIX_IN : MIX_SY, "v100", "v101", MIX_TEMPS)
#define MIX_RUN_B(T) asm volatile(T : "+{v[16:31]}"(sx), "+{v[48:63]}"(o), "+{v[64:79]}"(g), "+{v[86:87]}"(kc), "+{v[88:91]}"(ka), "+{v[96:99]}"(v1), \
                                      "+{v[104:107]}"(p1), [cnt] "=&s"(cnt) : MIX_Q, MIX_IN : MIX_SY, "v100", "v101", MIX_TEMPS)

template <bool gw>
DI void mix_compute(char* lds, const void* bsp, char* ws, int w, int head, int qb, int tp, int FT) {
  const int l = threadIdx.x & 63, r = l & 31, h = l >> 5;
  const int odd = head & 1, o0 = 5 * (head >> 1), oa = o0 + (odd ? 3 : 0), ob = oa + 1, oc = o0 + 2, hoff = odd * 8;
  const char* qkv = ws + OFF_QKV;
  const unsigned qs = (qb * 256 + w * 32 + r) * 16;
  const int ft = w & 1, tt = w >> 1;
  const int ttok = tp * 128 + tt * 32 + pi32(r), qtok = qb * 256 + w * 32 + pi32(r);
  auto ones = [&]() {
    for (int i = threadIdx.x; i < 6 * 128; i += 512) *(LAS unsigned*)((LAS char*)lds + M_KV + (i >> 7) * KV_SLOT + KV_ONES + (i & 127) * 4) = 0x3F803F80u;
    if (w == 7 && l < 2) *(LAS unsigned*)((LAS char*)lds + M_ZERO + l * 4) = 0u;
  };
  const unsigned lb = (unsigned)(size_t)lds;
  const int q = (l >> 2) & 3, p = l & 3;
  const int trow = 32 * tt + pi32(r);
  u32x4 at;
#pragma unroll
  for (int ks = 0; ks < 4; ++ks) at[ks] = lb + M_TR + trow * 128 + (((2 * ks + h) ^ ((trow >> 1) & 7)) << 4);
  f32x16 z1 = {}, g;
  if (gw) {
    const int zc = 32 * ft + pi32((l & 16) + 4 * p);
    const unsigned aw = (8 * h + q) * 128 + (((zc >> 3) ^ ((q >> 1) << 2)) << 4) + (zc & 4) * 2;
    float bs;
    bf16x8 t[2][4], f[2][4];
    auto rd = [&](int st, int u) {
      if (st == 1) wait_vm<0>();
      if (st == 0) bar_lgkm_c(); else bar();
      if (st == 1) bs = __builtin_bit_cast(float, (unsigned)((const unsigned short*)bsp)[ttok] << 16);
#pragma unroll
      for (int ks = 0; ks < 4; ++ks) { t[u][ks] = ld128((lptr)(size_t)(at[ks] + (z_t(st) - M_TR))); f[u][ks] = ldtr8((lptr)lds + aw + z_f(st) + ks * 2048, 512); }
      SB();
    };
    auto mm = [&](int u) {
#pragma unroll
      for (int ks = 0; ks < 4; ++ks) z1 = mfma16(f[u][ks], t[u][ks], z1);
      SB();
    };
    rd(0, 0);
#pragma unroll
    for (int st = 1; st < 9; st += 2) { rd(st, 1); mm(0); rd(st + 1, 0); mm(1); }
    rd(9, 1); ones(); mm(0); mm(1);
    gelu16(z1);
#pragma unroll
    for (int i = 0; i < 16; ++i) g[i] = bs;
    PIN(z1);
    asm volatile("" : "+{v[64:79]}"(g));
  } else {
    bar_lgkm(); wait_vm<0>();
#pragma unroll 1
    for (int st = 1; st < 10; ++st) bar();
    ones();
  }
  unsigned ak = lb + M_KV + h * 1088 + r * 16, akc = lb + M_KV + 2 * 1088 + (h ? 0 : r * 16 + hoff);
  unsigned av = lb + M_KV + ((l & 16) ? (p == 0 ? 2 * 1088 + 512 + hoff : KV_ONES) : (p & 1) * 1088 + 512 + (p >> 1) * 8) + h * 64 + q * 16;
  unsigned af = lb + M_FR + h * 1024 + (32 * ft + r) * 16;
  f32x16 sx, o;
  bf16x8 qa; s16x4 qc;
  u32x2 aq = {lb + M_Q + h * 4096 + (32 * w + r) * 16, h ? lb + M_ZERO : lb + M_Q + 8192 + (32 * w + r) * 16 + hoff};
  u32x4 ka, v1, p1; u32x2 kc; unsigned cnt;
  asm volatile("" : "+{v124}"(ak), "+{v125}"(akc), "+{v126}"(av), "+{v127}"(af), "+{v[128:131]}"(at), "+{v[100:101]}"(aq));
  bar_lgkm();
  const GAS float* gp = (const GAS float*)(ws + OFF_GATE);
  const float gq = __hip_atomic_load(gp + qtok, __ATOMIC_RELAXED, __HIP_MEMORY_SCOPE_AGENT);
  const float gt = gw ? __hip_atomic_load(gp + ttok, __ATOMIC_RELAXED, __HIP_MEMORY_SCOPE_AGENT) : 0.f;
  MIX_RUN_A(MIX_A0);
  if (gw) MIX_RUN_B(MIX_B1); else MIX_RUN_B0(MIX_B0);
  const rsrc_t rou = mk_rsrc(ws + OFF_OU, OU_BYTES);
  if (gw) {
    const f32x16 u = z1 * g * (1.0f - gt);
    const unsigned uo = (80 + 8 * FT + 4 * ft + h) * PLANE + (tp * 128 + tt * 32 + r) * 16;
    st128(rou, uo, pk8(u, 0)); st128(rou, uo + 2 * PLANE, pk8(u, 8));
  }
  float big = 0.f;
#pragma unroll
  for (int i = 0; i < 12; ++i) big += __builtin_fabsf(o[i]);
  const bool ok = o[12] > 8.881784197001252e-16f && o[12] < 1.2676506002282294e30f && big < 1.0e38f;
  auto put_o = [&](const f32x16& a) {
    const f32x16 t = a * (gq * __builtin_amdgcn_rcpf(a[12]));
    st128(rou, (h ? ob : oa) * PLANE + qs, pk8(t, 0));
    if (h == 0) __builtin_amdgcn_raw_buffer_store_b64(u32x2{pk2(t[8], t[9]), pk2(t[10], t[11])}, rou, oc * PLANE + qs + hoff, 0, 16);
  };
  if (__builtin_expect(__all(ok), 1)) put_o(o);
  else put_o(att_fallback(qa, qc, lds + w * KV_SLOT, qkv, oa, ob, oc, hoff, l));
  wait_vm<0>();
  bar_lgkm();
}

DI float lo16(unsigned v) { return __builtin_bit_cast(float, v << 16); }
DI float hi16(unsigned v) { return __builtin_bit_cast(float, v & 0xFFFF0000u); }

DI float reduce16(float (&s)[16]) {
#define RD_(i, j, ctl, m0, m1) "v_add_f32_dpp %" #i ", %" #i ", %" #i " " ctl " row_mask:0xf bank_mask:" m0 "\n\tv_add_f32_dpp %" #i ", %" #j ", %" #j " " ctl " row_mask:0xf bank_mask:" m1 "\n\t"
  asm volatile("s_nop 1\n\t"
               RD_(0, 8, "row_half_mirror", "0x5", "0xa") RD_(1, 9, "row_half_mirror", "0x5", "0xa") RD_(2, 10, "row_half_mirror", "0x5", "0xa") RD_(3, 11, "row_half_mirror", "0x5", "0xa")
               RD_(4, 12, "row_half_mirror", "0x5", "0xa") RD_(5, 13, "row_half_mirror", "0x5", "0xa") RD_(6, 14, "row_half_mirror", "0x5", "0xa") RD_(7, 15, "row_half_mirror", "0x5", "0xa")
               "s_nop 1\n\t"
               RD_(0, 4, "row_ror:8", "0x3", "0xc") RD_(1, 5, "row_ror:8", "0x3", "0xc") RD_(2, 6, "row_ror:8", "0x3", "0xc") RD_(3, 7, "row_ror:8", "0x3", "0xc")
               "s_nop 1\n\tv_permlane32_swap_b32 %0, %2\n\tv_permlane32_swap_b32 %1, %3\n\ts_nop 1\n\tv_add_f32 %0, %0, %2\n\tv_add_f32 %1, %1, %3\n\t"
               "s_nop 1\n\tv_permlane16_swap_b32 %0, %1\n\ts_nop 1\n\tv_add_f32 %0, %0, %1\n\t"
               "s_nop 1\n\tv_add_f32_dpp %0, %0, %0 quad_perm:[1,0,3,2] row_mask:0xf bank_mask:0xf\n\t"
               "s_nop 1\n\tv_add_f32_dpp %0, %0, %0 quad_perm:[2,3,0,1] row_mask:0xf bank_mask:0xf\n\ts_nop 1"
               : "+v"(s[0]), "+v"(s[1]), "+v"(s[2]), "+v"(s[3]), "+v"(s[4]), "+v"(s[5]), "+v"(s[6]), "+v"(s[7])
               : "v"(s[8]), "v"(s[9]), "v"(s[10]), "v"(s[11]), "v"(s[12]), "v"(s[13]), "v"(s[14]), "v"(s[15]));
#undef RD_
  return s[0];
}

DI void flag_set(rsrc_t rf, unsigned tag, int l) {
  if (l < 8) __builtin_amdgcn_raw_buffer_store_b32(tag, rf, FL_P + l * 1024 + blockIdx.x * 4, 0, 16);
}
DI void flag_wait(rsrc_t rf, unsigned tag, int l) {
  for (unsigned spin = 0;; ++spin) {
    const u32x4 c = __builtin_amdgcn_raw_buffer_load_b128(rf, FL_P + (blockIdx.x & 7) * 1024 + l * 16, 0, 16);
    if (__all(c[0] == tag && c[1] == tag && c[2] == tag && c[3] == tag) || spin > (1u << 20)) break;
    __builtin_amdgcn_s_sleep(1);
    asm volatile("" ::: "memory");
  }
  asm volatile("" ::: "memory");
}
DI void arrive(char* ws, rsrc_t rf, unsigned off, unsigned tag, int l) {
  const unsigned sh = (blockIdx.x >> 3) & 3;
  unsigned old = 0;
  if (l == 0) old = __hip_atomic_fetch_add((GAS unsigned*)(ws + OFF_FLAG + off + sh * 128), 1u, __ATOMIC_RELAXED, __HIP_MEMORY_SCOPE_AGENT);
  if (rfl(old) == 63u && l < 8) __builtin_amdgcn_raw_buffer_store_b32(tag, rf, off + 512 + l * 128 + sh * 4, 0, 16);
}
DI void await(rsrc_t rf, unsigned off, unsigned tag) {
  for (unsigned spin = 0;; ++spin) {
    const u32x4 c = __builtin_amdgcn_raw_buffer_load_b128(rf, 0, off + 512 + (blockIdx.x & 7) * 128, 16);
    if (rfl(c[0] == tag && c[1] == tag && c[2] == tag && c[3] == tag) || spin > (1u << 20)) break;
    __builtin_amdgcn_s_sleep(1);
    asm volatile("" ::: "memory");
  }
  asm volatile("" ::: "memory");
}

template <int KIND>
DI void mix_loader(const int ROLE, const bool HASG, char* lds, const void* x, const void* win, const void* wsp, char* ws, unsigned ev, bool inp, int head, int qb, int tp, int FT, int mt, int nt) {
  const int l = threadIdx.x & 63, lt = ROLE * 64 + l, blk = blockIdx.x;
  const unsigned lds0 = (unsigned)(size_t)lds;
  constexpr int NG = KIND != 0 ? 8 : 0;
  const rsrc_t rkv = mk_rsrc(ws + OFF_QKV, QKV_BYTES), rf = mk_rsrc(ws + OFF_FLAG, FL_BYTES);
  const rsrc_t r1 = KIND == 3 ? mk_rsrc(win, D * 1728 * 2) : mk_rsrc(x, S * D * 2);
  const rsrc_t rws = mk_rsrc(wsp, S * S * 2), r2 = KIND == 3 ? mk_rsrc(ws + OFF_Z2N, Z_BYTES) : rws;
  unsigned vk[3], vq[3], va[2], vb[2];
  if (KIND == 3) {
    va[0] = (l >> 3) * 3456 + FT * 128 + (((l & 7) ^ (((l >> 4) & 1) << 2)) << 4);
    vb[0] = (64 * FT + l < FH ? 64 * FT + l : 64 * FT + l - 32) * 16;
  } else if (KIND != 0) {
#pragma unroll
    for (int i = 0; i < 2; ++i) {
      const int row = 64 * (ROLE - 1) + 8 * i + (l >> 3); const unsigned c = ((l & 7) ^ ((row >> 1) & 7)) << 4;
      va[i] = (tp * 128 + row) * 1280 + c; vb[i] = (tp * 128 + row) * 4096 + c;
    }
  }
  auto stage1 = [&](int st) {
    if (!HASG) return;
#pragma unroll
    for (int i = 0; i < NG; ++i) {
      if (KIND == 3) dma16(lds0 + z_f(st) + i * 1024, va[0], r1, (st * 64 + i * 8) * 3456);
      else dma16(lds0 + z_t(st) + (ROLE - 1) * 8192 + i * 1024, va[i & 1], r1, st * 128 + (i >> 1) * 16 * 1280);
    }
  };
  auto stage2 = [&](int st) {
    const int k = (st - 10 + 4 * (blk & 7)) & 31;
    if (!HASG) return;
#pragma unroll
    for (int i = 0; i < NG; ++i) {
      if (KIND == 3) dma16_sc1(lds0 + M_FR + (st & 3) * 8192 + i * 1024, vb[0], r2, (k * 8 + i) * ZROW);
      else dma16(lds0 + M_TR + (st & 3) * 16384 + (ROLE - 1) * 8192 + i * 1024, vb[i & 1], r2, k * 128 + (i >> 1) * 16 * 4096);
    }
  };
  const rsrc_t rw = mk_rsrc(ws + OFF_WT + (size_t)nt * WT_TILE, WT_TILE);
  auto wpre = [&](int at) {
#pragma unroll
    for (int i = 0; i < 8; ++i) dma16_sc1(lds0 + at + i * 1024, l * 16, rw, at - O_W + i * 1024);
  };
  auto wslot = [&](int sl) { wpre(KIND == 3 ? M_FR + sl * 8192 : M_TR + sl * 16384 + (ROLE - 1) * 8192); };
  if (inp) {
    wait_vm<10>(); bar(); stage1(0); stage1(1);
    if (!HASG) wait_vm<0>();
    wait_vm<2 * NG>(); bar(); stage1(2); stage1(3);
    bar();
  } else {
    bar();
    stage1(0); stage1(1); stage1(2); stage1(3);
  }
  stage1(4);
  if (KIND == 0 && l == 0) *(volatile LAS unsigned*)((LAS char*)lds + M_GO) = 0u;
  if (KIND == 0 && blk == 0 && l < 8) __builtin_amdgcn_raw_buffer_store_b32(0u, rf, (l < 4 ? FL_L : FL_M) + (l & 3) * 128, 0, 16);
  if (!HASG) wait_vm<0>();
  asm volatile("s_waitcnt vmcnt(%1)" : "+v"(ev) : "i"(4 * NG) : "memory");
  const unsigned cnt = rfl(ev), tag = cnt + 1u + ((unsigned)dispatch_id() << 8);
  bar_lgkm();
  stage1(5);
  int e = 0, es = 0;
  auto kv = [&]() {
#pragma unroll
    for (int i = 0; i < 3; ++i) dma16_sc1(lds0 + M_KV + es * KV_SLOT + i * 1088, vk[i], rkv, e * 512);
    ++e; es = es == 5 ? 0 : es + 1;
  };
  if (KIND == 0) {
    const int odd = head & 1, o0 = 5 * (head >> 1);
#pragma unroll
    for (int i = 0; i < 3; ++i) {
      const int oct = i == 2 ? o0 + 2 : o0 + (odd ? 3 : 0) + i;
      vk[i] = l < 32 ? (80 + oct) * PLANE + l * 16 : (160 + oct) * PLANE + (l - 32) * 16 - 512;
      vq[i] = oct * PLANE + (qb * 256 + l) * 16;
    }
    u32x4 c;
    const unsigned vf = FL_P + (blk & 7) * 1024 + l * 16;
    bar();
    flag_set(rf, tag, l);
#pragma unroll 1
    for (int st = 2; st < 8; ++st) bar();
    asm volatile("buffer_load_dwordx4 %0, %1, %2, 0 offen sc1" : "=v"(c) : "v"(vf), "s"(rf) : "memory");
    bar(); bar();
    asm volatile("s_waitcnt vmcnt(0)" : "+v"(c) :: "memory");
    if (!__all(c[0] == tag && c[1] == tag && c[2] == tag && c[3] == tag)) flag_wait(rf, tag, l);
    if (blk == 0 && l == 0) __builtin_amdgcn_raw_buffer_store_b32(cnt + 1u, rf, 0, 0, 16);
    kv(); kv();
#pragma unroll
    for (int i = 0; i < 12; ++i) dma16_sc1(lds0 + M_Q + i * 1024, vq[i >> 2], rkv, (i & 3) * 1024);
    kv();
    if (l == 0) *(volatile LAS unsigned*)((LAS char*)lds + M_GO) = 1u;
    kv();
  } else {
    wait_vm<4 * NG>(); bar();
    wait_vm<3 * NG>(); bar(); stage1(6);
    wait_vm<3 * NG>(); bar(); stage1(7);
    wait_vm<3 * NG>(); bar(); stage1(8);
    wait_vm<3 * NG>(); bar();
    wait_vm<2 * NG>(); bar(); stage1(9);
    wait_vm<2 * NG>(); bar();
    wait_vm<NG>(); bar();
    wait_vm<0>(); bar();
    for (unsigned spin = 0; *(volatile LAS unsigned*)((LAS char*)lds + M_GO) == 0u && spin < (1u << 24); ++spin) __builtin_amdgcn_s_sleep(1);
    asm volatile("" ::: "memory");
  }
  const rsrc_t rzr = mk_rsrc(ws + OFF_Z2R + (size_t)blk * ZROW, ZROW);
  u32x4 zr[4], piv;
  if (KIND == 0) { wait_vm<3>(); bar(); kv(); kv(); }
  asm volatile("s_nop 4\n\tbuffer_load_dwordx4 %0, %5, %7, 0 offen sc1\n\tbuffer_load_dwordx4 %1, %5, %7, %8 offen sc1\n\tbuffer_load_dwordx4 %2, %5, %7, %9 offen sc1\n\t"
               "buffer_load_dwordx4 %3, %6, %7, 0 offen sc1\n\tbuffer_load_dwordx4 %4, off, %7, 0 sc1"
               : "=&v"(zr[0]), "=&v"(zr[1]), "=&v"(zr[2]), "=&v"(zr[3]), "=&v"(piv)
               : "v"(lt * 16), "v"(lt < FH - 768 ? lt * 16 + 12288 : 0), "s"(rzr), "s"(4096), "s"(8192) : "memory");
  if (KIND != 0) bar();
  if (KIND == 0) wait_vm<6 + 5>();
  bar();
  if (KIND == 0) { kv(); kv(); }
  asm volatile("s_waitcnt vmcnt(%5)" : "+v"(zr[0]), "+v"(zr[1]), "+v"(zr[2]), "+v"(zr[3]), "+v"(piv) : "i"(KIND == 0 ? 6 : 0) : "memory");
  f32x2 d[4][4], s1[4] = {}, s2[4] = {};
#pragma unroll
  for (int t = 0; t < 4; ++t) {
    const f32x2 pv = {lo16(piv[t]), hi16(piv[t])};
#pragma unroll
    for (int i = 0; i < 4; ++i) {
      d[i][t] = f32x2{lo16(zr[i][t]), hi16(zr[i][t])} - pv;
      s1[t] += d[i][t]; s2[t] = __builtin_elementwise_fma(d[i][t], d[i][t], s2[t]);
    }
  }
  float s[16];
#pragma unroll
  for (int t = 0; t < 8; ++t) { s[t] = s1[t >> 1][t & 1]; s[8 + t] = s2[t >> 1][t & 1]; }
  const float part = reduce16(s);
  if ((l & 3) == 0) *(LAS float*)((LAS char*)lds + M_LN + ROLE * 64 + ((l >> 4) + ((l & 4) << 1) + ((l & 8) >> 1)) * 4) = part;
  bar_lgkm();
  float a = *(const LAS float*)((lptr)lds + M_LN + l * 4);
  if (KIND == 0) kv();
  {
    const rsrc_t rzo = mk_rsrc(ws + OFF_Z2N + (size_t)blk * ZROW, ZROW);
    { float c = a;
      asm volatile("s_nop 1\n\tv_permlane32_swap_b32 %0, %1\n\ts_nop 1\n\tv_add_f32 %0, %0, %1\n\tv_mov_b32 %1, %0\n\ts_nop 1\n\t"
                   "v_permlane16_swap_b32 %0, %1\n\ts_nop 1\n\tv_add_f32 %0, %0, %1\n\ts_nop 1" : "+v"(a), "+v"(c)); }
    const float b = __builtin_bit_cast(float, __builtin_amdgcn_update_dpp(0, __builtin_bit_cast(int, a), 0x128, 0xf, 0xf, false));
    const float mu = a * (1.0f / FH), rs = __builtin_amdgcn_rsqf(__builtin_fmaxf(b * (1.0f / FH) - mu * mu, 0.f) + 1e-5f), nm = -mu * rs;
    u32x4 o[4];
#pragma unroll
    for (int t = 0; t < 4; ++t) {
      const f32x2 r2v = {__builtin_bit_cast(float, __builtin_amdgcn_readlane(__builtin_bit_cast(int, rs), 2 * t)), __builtin_bit_cast(float, __builtin_amdgcn_readlane(__builtin_bit_cast(int, rs), 2 * t + 1))};
      const f32x2 n2v = {__builtin_bit_cast(float, __builtin_amdgcn_readlane(__builtin_bit_cast(int, nm), 2 * t)), __builtin_bit_cast(float, __builtin_amdgcn_readlane(__builtin_bit_cast(int, nm), 2 * t + 1))};
#pragma unroll
      for (int i = 0; i < 4; ++i) { const f32x2 y = __builtin_elementwise_fma(d[i][t], r2v, n2v); o[i][t] = pk2(y[0], y[1]); }
    }
#pragma unroll
    for (int i = 0; i < 4; ++i) __builtin_amdgcn_raw_buffer_store_b128(o[i], rzo, lt * 16 + i * 4096, 0, 16);
  }
  if (KIND == 0) {
    wait_vm<10>(); bar(); kv(); kv();
    wait_vm<6>(); bar(); kv();
    wait_vm<3>(); bar();
    kv(); kv();
    wait_vm<6>(); bar(); kv();
    wait_vm<3>(); bar(); kv(); kv();
#pragma unroll 1
    for (int s2i = 7; s2i < 39; s2i += 2) {
      wait_vm<6>(); bar(); kv();
      wait_vm<3>(); bar(); kv(); kv();
    }
    wait_vm<6>(); bar();
    wait_vm<3>(); bar();
    wait_vm<0>(); bar();
  } else {
    bar();
    wait_vm<0>(); bar();
    unsigned one = 1;
    const unsigned sh = (blk >> 3) & 3;
    if (KIND == 1 && ROLE == 1 && l == 0) asm volatile("buffer_atomic_add %0, off, %1, %2 sc0" : "+v"(one) : "s"(rf), "s"(FL_L + sh * 128) : "memory");
    bar();
    if (KIND == 1 && ROLE == 1) {
      asm volatile("s_waitcnt vmcnt(0)" : "+v"(one) :: "memory");
      if (rfl(one) == 63u && l < 8) __builtin_amdgcn_raw_buffer_store_b32(tag, rf, FL_L + 512 + l * 128 + sh * 4, 0, 16);
    }
    bar(); if (KIND == 1) stage2(10);
    bar(); if (KIND == 1) stage2(11);
    bar(); if (KIND == 1) stage2(12);
    u32x4 c;
    if (KIND == 3) asm volatile("buffer_load_dwordx4 %0, off, %1, %2 sc1" : "=v"(c) : "s"(rf), "s"(FL_L + 512 + (blk & 7) * 128) : "memory");
    bar();
    if (KIND == 3) {
      asm volatile("s_waitcnt vmcnt(0)" : "+v"(c) :: "memory");
      if (HASG && !rfl(c[0] == tag && c[1] == tag && c[2] == tag && c[3] == tag)) await(rf, FL_L, tag);
      stage2(10);
    }
    bar();
    if (KIND == 3) { stage2(11); stage2(12); }
#pragma unroll 1
    for (int s2i = 10; s2i < 39; ++s2i) { wait_vm<2 * NG>(); bar(); stage2(s2i + 3); }
    constexpr int W0 = KIND == 3 ? 8 : 0;
    if (KIND == 3) wpre(O_W);
    wait_vm<2 * NG + W0>(); bar(); wslot(2);
    wait_vm<NG + W0 + 8>(); bar(); wslot(3);
    wait_vm<W0 + 16>(); bar(); wslot(0);
  }
  bar();
  bar();
  if (KIND == 0) { arrive(ws, rf, FL_M, tag, l); await(rf, FL_M, tag); } else wslot(1);
  bar();
  if (ROLE < 2) {
    const rsrc_t ra = mk_rsrc(ws + OFF_OU, OU_BYTES);
    const unsigned vo = (mt * 64 + l) * 16;
    int sa = 0;
    auto issue = [&](int st) {
#pragma unroll
      for (int i = 0; i < 4; ++i) dma16_sc1(lds0 + sa + (4 * ROLE + i) * 1024, vo, ra, (st * 8 + 4 * ROLE + i) * PLANE);
      sa = sa == 6 * 8192 ? 0 : sa + 8192;
    };
    issue(0); issue(1); issue(2); issue(3);
#pragma unroll 1
    for (int st = 0; st < 20; st += 2) { wait_vm<8>(); bar(); issue(st + 4); issue(st + 5); }
    wait_vm<8>(); bar(); wait_vm<0>(); bar();
  } else {
    auto issue = [&](int st) {
#pragma unroll
      for (int i = 0; i < 5; ++i) dma16_sc1(lds0 + O_W + ((st % 10) * 10 + (ROLE - 2) * 5 + i) * 1024, l * 16, rw, (st * 10 + (ROLE - 2) * 5 + i) * 1024);
    };
    wait_vm<0>(); bar();
    bar(); issue(10);
#pragma unroll
    for (int S2 = 2; S2 < 8; ++S2) { wait_vm<20>(); bar(); issue(2 * S2 + 7); issue(2 * S2 + 8); }
    wait_vm<20>(); bar(); issue(23);
    wait_vm<20>(); bar(); wait_vm<10>(); bar(); wait_vm<0>(); bar();
  }
}

extern "C" __global__ __launch_bounds__(768, 3) void k_mix(const void* x, const void* wqkv, const void* win, const void* gw, const void* wo, const void* wout,
                                                           const void* wsp, const void* bsp, char* ws, void* out) {
  __shared__ __attribute__((aligned(16))) char lds[M_BYTES];
  const int xcd = blockIdx.x & 7, idx = blockIdx.x >> 3, w = rfl(threadIdx.x >> 6);
  const int panel = 8 * (xcd >> 2) + (idx & 7), ct = 4 * (xcd & 3) + (idx >> 3);
  const int head = 4 * xcd + (idx >> 3), qb = idx & 7;
  const bool hasg = idx < 28;
  const int tp = 2 * xcd + (idx & 1), FT = idx >> 1;
  const int mt = 8 * (xcd >> 1) + (idx & 7), nt = 4 * (xcd & 1) + (idx >> 3);
  if (w >= 8) {
    __builtin_amdgcn_s_setprio(3);
    unsigned ev;
    { const rsrc_t rf = mk_rsrc(ws + OFF_FLAG, FL_BYTES); asm volatile("s_nop 4\n\tbuffer_load_dword %0, off, %1, 0 sc1" : "=v"(ev) : "s"(rf) : "memory"); }
    if (ct == 14) proj_loader<true>(lds, x, wqkv, win, gw, w - 8, panel, ct);
    else if (ct != 15) proj_loader<false>(lds, x, wqkv, win, gw, w - 8, panel, ct);
    if (w == 8) mix_loader<0>(0, true, lds, x, win, wsp, ws, ev, ct != 15, head, qb, tp, FT, mt, nt);
    else if (w == 11) mix_loader<3>(3, hasg, lds, x, win, wsp, ws, ev, ct != 15, head, qb, tp, FT, mt, nt);
    else mix_loader<1>(w - 8, hasg, lds, x, win, wsp, ws, ev, ct != 15, head, qb, tp, FT, mt, nt);
    return;
  }
  if (ct == 15) { pack_weights(lds, wo, wout, ws, panel); bar_lgkm(); }
  else if (w >= 4) {
#pragma unroll 1
    for (int i = 0; i < 11; ++i) bar();
  }
  else if (ct == 14) proj_body<true>(lds, ws, w, panel, ct);
  else proj_body<false>(lds, ws, w, panel, ct);
  if (hasg && 2 * FT + (w & 1) < 27) mix_compute<true>(lds, bsp, ws, w, head, qb, tp, FT); else mix_compute<false>(lds, bsp, ws, w, head, qb, tp, FT);
  if (w < 4) out_compute<3>(lds, ws, out, w, mt, nt); else out_compute<2>(lds, ws, out, w, mt, nt);
}

extern "C" void kernel_launch(void* const* d_in, const int* in_sizes, int n_in, void* d_out, int out_size, void* d_ws, size_t ws_size, hipStream_t stream) {
  char* ws = (char*)d_ws;
  k_mix<<<256, 768, 0, stream>>>(d_in[0], d_in[1], d_in[4], d_in[8], d_in[2], d_in[7], d_in[5], d_in[6], ws, d_out);
}
```
